```python
import torch

def _softmax(x, axis=-1):
    return torch.softmax(x, dim=axis)
def _gelu(x):
    return torch.nn.functional.gelu(x, approximate="tanh")
def _silu(x):
    return torch.nn.functional.silu(x)
def _sigmoid(x):
    return torch.sigmoid(x)
def _layernorm(x, axis=-1, eps=1e-5):
    m = x.mean(dim=axis, keepdim=True)
    v = x.var(dim=axis, keepdim=True, unbiased=False)
    return (x - m) / torch.sqrt(v + eps)
def _scatter_add(tgt, idx, src, axis=0):
    return tgt.clone().index_add_(axis, idx, src)
def _segment_sum(src, seg, n):
    out = torch.zeros((n,) + src.shape[1:], dtype=src.dtype)
    return out.index_add_(0, seg, src)
def _pad(x, pw):
    flat = tuple(v for pair in reversed(pw) for v in pair)
    return torch.nn.functional.pad(x, flat)

kernel_name = 'sequential_preLN__sliding_block__glu_gated_bilinear__prompt'

def setup_inputs():

    _w = (lambda a: (lambda t: t.to(torch.bfloat16) if t.is_floating_point() else t)(torch.as_tensor(a)))
    def _A_setup():
        B, S, D = 1, 2048, 640
        return {
            "x": _w((lambda *s: torch.randn(*s, dtype=torch.bfloat16) * 0.05)(B, S, D)),
            "wqkv": _w((lambda *s: torch.randn(*s, dtype=torch.bfloat16) * 0.05)(D, 3 * D)),
            "wo": _w((lambda *s: torch.randn(*s, dtype=torch.bfloat16) * 0.05)(D, D)),
        }
    _A_in = _A_setup()

    def _B_setup():
        B, S, D, F = 1, 2048, 640, 1536
        return {
            "x": _w((lambda *s: torch.randn(*s, dtype=torch.bfloat16) * 0.05)(B, S, D)),
            "w_gate": _w((lambda *s: torch.randn(*s, dtype=torch.bfloat16) * 0.8)(D, F) * (D ** -0.5)),
            "w_up": _w((lambda *s: torch.randn(*s, dtype=torch.bfloat16) * 0.8)(D, F) * (D ** -0.5)),
            "w_down": _w((lambda *s: torch.randn(*s, dtype=torch.bfloat16) * 0.8)(F, D) * (F ** -0.5)),
        }
    _B_in = _B_setup()

    return {
        "A_x": _A_in["x"],
        "A_wqkv": _A_in["wqkv"],
        "A_wo": _A_in["wo"],
        "B_x": _B_in["x"],
        "B_w_gate": _B_in["w_gate"],
        "B_w_up": _B_in["w_up"],
        "B_w_down": _B_in["w_down"],
    }

def reference(A_x, A_wqkv, A_wo, B_x, B_w_gate, B_w_up, B_w_down):
    def _A_ref(x, wqkv, wo):
        B, S, D = x.shape
        H = 32; Dh = D // H; W = 256
        qkv = (lambda x, *d: x.permute(*d))((x @ wqkv).reshape(B, S, 3, H, Dh), 2, 0, 3, 1, 4)
        q, k, v = qkv[0], qkv[1], qkv[2]
        scores = torch.einsum("bhqd,bhkd->bhqk", q, k) * (Dh ** -0.5)
        idx = torch.arange(S)
        mask = (idx[:, None] - idx[None, :] >= 0) & (idx[:, None] - idx[None, :] < W)
        scores = torch.where(mask[None, None], scores, -1e9)
        p = _softmax(scores, axis=-1)
        o = torch.einsum("bhqk,bhkd->bhqd", p, v)
        return (lambda x, *d: x.permute(*d))(o, 0, 2, 1, 3).reshape(B, S, D) @ wo

    def _B_ref(x, w_gate, w_up, w_down):
        g = torch.einsum("bsd,df->bsf", x, w_gate)
        u = torch.einsum("bsd,df->bsf", x, w_up)
        h = g * u
        return torch.einsum("bsf,fd->bsd", h, w_down)

    x = A_x
    h = x + _A_ref(_layernorm(x, axis=-1), A_wqkv, A_wo)
    h2 = h + _B_ref(_layernorm(h, axis=-1), B_w_gate, B_w_up, B_w_down)
    return h2
```

```cpp
#include <hip/hip_runtime.h>
#include <hip/hip_bf16.h>
typedef unsigned short u16;
__device__ __forceinline__ float bf2f(u16 b){ return __uint_as_float(((unsigned)b)<<16); }
__device__ __forceinline__ u16 f2bf(float f){ unsigned u=__float_as_uint(f); u += 0x7fffu + ((u>>16)&1u); return (u16)(u>>16); }
constexpr int S=2048, D=640, F=1536, H=32, DH=20, W=256;

__global__ void ln_k(const u16* x, u16* y){
  int r=blockIdx.x; int t=threadIdx.x;
  float v[10]; float s=0;
  for(int i=0;i<10;i++){ v[i]=bf2f(x[r*D+t+64*i]); s+=v[i]; }
  for(int o=32;o;o>>=1) s+=__shfl_xor(s,o);
  float m=s/D; float q=0;
  for(int i=0;i<10;i++){ float d=v[i]-m; q+=d*d; }
  for(int o=32;o;o>>=1) q+=__shfl_xor(q,o);
  float rs=rsqrtf(q/D+1e-5f);
  for(int i=0;i<10;i++) y[r*D+t+64*i]=f2bf((v[i]-m)*rs);
}
__global__ void gemm_k(const u16* A,const u16* B,u16* C,const u16* res,int M,int N,int K){
  int n=blockIdx.x*64+threadIdx.x; int m=blockIdx.y;
  float acc=0;
  for(int k=0;k<K;k++) acc+=bf2f(A[m*K+k])*bf2f(B[k*N+n]);
  float r=bf2f(f2bf(acc));
  if(res) r+=bf2f(res[m*N+n]);
  C[m*N+n]=f2bf(r);
}
__global__ void attn_k(const u16* qkv,u16* o){
  int q=blockIdx.x*64+threadIdx.x; int h=blockIdx.y;
  float qv[DH]; for(int d=0;d<DH;d++) qv[d]=bf2f(qkv[q*3*D+h*DH+d]);
  float m=-1e30f,l=0,acc[DH]; for(int d=0;d<DH;d++) acc[d]=0;
  int k0=q-W+1; if(k0<0)k0=0;
  for(int k=k0;k<=q;k++){
    float s=0; for(int d=0;d<DH;d++) s+=qv[d]*bf2f(qkv[k*3*D+D+h*DH+d]);
    s*=0.22360679775f;
    float mn=fmaxf(m,s); float a=__expf(m-mn); float p=__expf(s-mn);
    l=l*a+p; for(int d=0;d<DH;d++) acc[d]=acc[d]*a+p*bf2f(qkv[k*3*D+2*D+h*DH+d]);
    m=mn;
  }
  for(int d=0;d<DH;d++) o[q*D+h*DH+d]=f2bf(acc[d]/l);
}
__global__ void mul_k(const u16* g,const u16* u,u16* h,int n){ int i=blockIdx.x*256+threadIdx.x; if(i<n) h[i]=f2bf(bf2f(g[i])*bf2f(u[i])); }

extern "C" void kernel_launch(void* const* d_in, const int* in_sizes, int n_in, void* d_out, int out_size, void* d_ws, size_t ws_size, hipStream_t stream) {
  const u16* x=(const u16*)d_in[0]; const u16* wqkv=(const u16*)d_in[1]; const u16* wo=(const u16*)d_in[2];
  const u16* wg=(const u16*)d_in[4]; const u16* wu=(const u16*)d_in[5]; const u16* wd=(const u16*)d_in[6];
  u16* ws=(u16*)d_ws;
  u16* xn=ws; u16* qkv=xn+S*D; u16* o=qkv+S*3*D; u16* h=o+S*D; u16* hn=h+S*D; u16* g=hn+S*D; u16* u=g+S*F; u16* hh=u+S*F;
  ln_k<<<S,64,0,stream>>>(x,xn);
  gemm_k<<<dim3(3*D/64,S),64,0,stream>>>(xn,wqkv,qkv,nullptr,S,3*D,D);
  attn_k<<<dim3(S/64,H),64,0,stream>>>(qkv,o);
  gemm_k<<<dim3(D/64,S),64,0,stream>>>(o,wo,h,x,S,D,D);
  ln_k<<<S,64,0,stream>>>(h,hn);
  gemm_k<<<dim3(F/64,S),64,0,stream>>>(hn,wg,g,nullptr,S,F,D);
  gemm_k<<<dim3(F/64,S),64,0,stream>>>(hn,wu,u,nullptr,S,F,D);
  mul_k<<<S*F/256,256,0,stream>>>(g,u,hh,S*F);
  gemm_k<<<dim3(D/64,S),64,0,stream>>>(hh,wd,(u16*)d_out,h,S,D,F);
}
```

```cpp
#include <hip/hip_runtime.h>
#include <stdint.h>

typedef unsigned long long u64;
typedef __attribute__((ext_vector_type(8))) short bf16x8;
typedef __attribute__((ext_vector_type(4))) short s16x4;
typedef __attribute__((ext_vector_type(2))) __bf16 bf16x2;
typedef __attribute__((ext_vector_type(16))) float f32x16;
typedef __attribute__((ext_vector_type(4))) float f32x4;
typedef __attribute__((ext_vector_type(2))) float f32x2;
typedef __attribute__((ext_vector_type(4))) unsigned u32x4;
typedef __attribute__((ext_vector_type(2))) unsigned u32x2;
#define LAS __attribute__((address_space(3)))
#define DI __device__ __forceinline__

constexpr unsigned WS_EPOCH = 0;
constexpr unsigned WS_F1 = 4096, WS_F2 = WS_F1 + 256 * 128, WS_F3 = WS_F2 + 256 * 128, WS_F4 = WS_F3 + 256 * 128;
constexpr unsigned WS_CSUM = WS_F4 + 256 * 128;
constexpr unsigned WS_STAT = WS_CSUM + 16 * 768;
constexpr unsigned WS_QKV = 1u << 20;
constexpr unsigned WS_O = WS_QKV + 16 * 240 * 2048;
constexpr unsigned WS_H = WS_O + 16 * 80 * 2048;
constexpr unsigned WS_HH = WS_H + 16 * 80 * 2048;
constexpr unsigned WS_END = WS_HH + 16 * 192 * 2048;
constexpr unsigned WS_DIAG = 32u << 20;

constexpr unsigned L_STAT = 0, L_CS = 1024;
constexpr unsigned L_ONES = 0, L_CSUM4 = 512;
constexpr unsigned L_OST = 1984;
constexpr unsigned L_P1 = 32768;
constexpr unsigned L_K = 12224, L_V = L_K + 5 * 6144, VSTR = 6208;
constexpr unsigned L_W3 = 61440;
constexpr unsigned L_RING = 12224, RSLOT = 24608;
constexpr unsigned UPOFF = 12288 + 32;

constexpr float QSCALE = 0.22360679774997896f * 1.4426950408889634f;

#define MFMA32(a, b, c) __builtin_amdgcn_mfma_f32_32x32x16_bf16((a), (b), (c), 0, 0, 0)
#define MFMA16(a, b, c) __builtin_amdgcn_mfma_f32_16x16x32_bf16((a), (b), (c), 0, 0, 0)
#define BAR() asm volatile("s_waitcnt lgkmcnt(0)\n\ts_barrier" ::: "memory")
#define RAWBAR() do { __builtin_amdgcn_sched_barrier(0); asm volatile("s_barrier" ::: "memory"); __builtin_amdgcn_sched_barrier(0); } while (0)
#define OPAQUE(v) asm volatile("" : "+v"(v))
#define SGB(m, n) __builtin_amdgcn_sched_group_barrier((m), (n), 0)
#define WAITV(n) asm volatile("s_waitcnt vmcnt(%0)" ::"i"(n) : "memory")

#define STAMP(i) do {} while (0)

DI void glds(unsigned voff, const char* sbase, unsigned ldst) {
  unsigned sv;
  asm volatile("s_mov_b32 %0, m0\n\ts_mov_b32 m0, %3\n\ts_nop 0\n\tglobal_load_lds_dwordx4 %1, %2\n\ts_mov_b32 m0, %0"
               : "=&s"(sv) : "v"(voff), "s"(sbase), "s"(ldst) : "memory");
}
DI void glds_sc1(unsigned voff, const char* sbase, unsigned ldst) {
  unsigned sv;
  asm volatile("s_mov_b32 %0, m0\n\ts_mov_b32 m0, %3\n\ts_nop 0\n\tglobal_load_lds_dwordx4 %1, %2 sc1\n\ts_mov_b32 m0, %0"
               : "=&s"(sv) : "v"(voff), "s"(sbase), "s"(ldst) : "memory");
}
#define ST128(v, voff, soff) __builtin_amdgcn_raw_buffer_store_b128((v), rs, (unsigned)(voff) + (unsigned)(soff), 0, 16)
#define ST64(v, voff, soff) __builtin_amdgcn_raw_buffer_store_b64((v), rs, (unsigned)(voff) + (unsigned)(soff), 0, 16)
DI bf16x8 ld_b128(LAS char* p) { return *(LAS bf16x8*)p; }
DI s16x4 ld_tr(LAS char* p) { return __builtin_amdgcn_ds_read_tr16_b64_v4i16((LAS s16x4*)p); }
DI bf16x8 ld_tr2(LAS char* p0, LAS char* p1) {
  s16x4 a = ld_tr(p0), b = ld_tr(p1);
  return __builtin_shufflevector(a, b, 0, 1, 2, 3, 4, 5, 6, 7);
}
DI unsigned pk2(float lo, float hi) {
  f32x2 v = {lo, hi};
  return __builtin_bit_cast(unsigned, __builtin_convertvector(v, bf16x2));
}
DI float bflo(unsigned w) { return __uint_as_float(w << 16); }
DI float bfhi(unsigned w) { return __uint_as_float(w & 0xffff0000u); }
DI float dot2(unsigned a, unsigned b, float c) {
  return __builtin_amdgcn_fdot2_f32_bf16(__builtin_bit_cast(bf16x2, a), __builtin_bit_cast(bf16x2, b), c, false);
}
DI float xor32(float v) {
  auto r = __builtin_amdgcn_permlane32_swap(__float_as_uint(v), __float_as_uint(v), false, false);
  return (threadIdx.x & 32) ? __uint_as_float(r[0]) : __uint_as_float(r[1]);
}
DI u64 mix64(u64 z) {
  z += 0x9E3779B97F4A7C15ull; z = (z ^ (z >> 30)) * 0xBF58476D1CE4E5B9ull; z = (z ^ (z >> 27)) * 0x94D049BB133111EBull;
  return z ^ (z >> 31);
}
DI u64 ld_flag(const char* p) { return __hip_atomic_load((const u64*)p, __ATOMIC_RELAXED, __HIP_MEMORY_SCOPE_AGENT); }
DI void st_flag(char* p, u64 v) { __hip_atomic_store((u64*)p, v, __ATOMIC_RELAXED, __HIP_MEMORY_SCOPE_AGENT); }
DI void wait_flags(const char* p, u64 epoch) {
  for (unsigned spins = 0; spins < (1u << 20); ++spins) {
    u64 v = ld_flag(p);
    if (__all(v == epoch)) break;
    __builtin_amdgcn_s_sleep(1);
  }
  asm volatile("" ::: "memory");
}

__global__ __launch_bounds__(512, 2) void fused_block(const char* __restrict__ x, const char* __restrict__ wqkv, const char* __restrict__ wo,
                                                      const char* __restrict__ wg, const char* __restrict__ wu, const char* __restrict__ wd,
                                                      char* __restrict__ out, char* ws) {
  __shared__ __attribute__((aligned(16))) char smem_[163840];
  LAS char* sm = (LAS char*)smem_;
  const unsigned sm0 = (unsigned)(uintptr_t)sm;
  const int tid = threadIdx.x, lane = tid & 63;
  const int wid = __builtin_amdgcn_readfirstlane(tid >> 6);
  const int b = blockIdx.x, g = 2 * (b & 7) + (b >> 7), c = (b >> 3) & 15;
  const int wgid = g * 16 + c;
  const auto rs = __builtin_amdgcn_make_buffer_rsrc(ws, 0, WS_END, 0x00020000);
  const bool isC = wid < 4;
  const int l31 = lane & 31, hi = lane >> 5, G = lane >> 4, q4 = (lane & 15) >> 2, p4 = lane & 3, l15 = lane & 15;
  STAMP(0);

  u64 epoch = 0;
  if (isC) {
    u64 e = mix64(ld_flag(ws + WS_EPOCH));
    unsigned e0 = __builtin_amdgcn_readfirstlane((unsigned)e), e1 = __builtin_amdgcn_readfirstlane((unsigned)(e >> 32));
    epoch = ((u64)e1 << 32) | e0;
  }

  if (c < 15) {
    if (!isC) {
      const int lw = wid - 4;
      unsigned vx0 = (lane >> 3) * 1280 + (((lane & 7) ^ (lane >> 4)) << 4);
      unsigned vx1 = (lane >> 3) * 1280 + (((lane & 7) ^ (4 + (lane >> 4))) << 4);
      unsigned vw = (lane >> 4) * 3840 + (((lane & 15) ^ ((lane >> 4) << 2)) << 4);
      const char* xb = x + (size_t)(128 * g + 32 * lw) * 1280;
      const char* wb = wqkv + (size_t)(16 * lw) * 3840 + c * 256;
      auto issue = [&](int s) {
        const unsigned slot = sm0 + L_P1 + (s & 3) * 32768;
#pragma unroll
        for (int i = 0; i < 4; ++i) {
          glds((i & 1) ? vx1 : vx0, xb + i * 8 * 1280 + s * 128, slot + (32 * lw + 8 * i) * 128);
          glds(vw, wb + (size_t)(64 * s + 4 * i) * 3840, slot + 16384 + (16 * lw + 4 * i) * 256);
        }
      };
      issue(0); issue(1); issue(2); issue(3);
      float s1 = 0.f, s2 = 0.f, cs = 0.f;
      const unsigned ones = 0x3f803f80u;
      const unsigned ax = L_P1 + (32 * lw + l31) * 128 + ((hi ^ ((l31 >> 1) & 7)) << 4);
      const unsigned aw = L_P1 + 16384 + (8 * hi + q4) * 256 + (((4 * lw + 2 * (G & 1) + (p4 >> 1)) ^ (q4 << 2)) << 4) + 8 * (p4 & 1);
      for (int s = 0; s < 10; ++s) {
        if (s == 0) WAITV(24); else if (s < 8) WAITV(16); else if (s == 8) WAITV(8); else WAITV(0);
        BAR();
        if (s >= 1 && s + 3 < 10) issue(s + 3);
        const unsigned so = (s & 3) * 32768;
#pragma unroll
        for (int ks = 0; ks < 4; ++ks) {
          u32x4 v = __builtin_bit_cast(u32x4, ld_b128(sm + ((ax ^ (ks * 32)) + so)));
#pragma unroll
          for (int j = 0; j < 4; ++j) { s1 = dot2(v[j], ones, s1); s2 = dot2(v[j], v[j], s2); }
          u32x4 w = __builtin_bit_cast(u32x4, ld_tr2(sm + (aw + so + ks * 4096), sm + (aw + so + ks * 4096 + 1024)));
#pragma unroll
          for (int j = 0; j < 4; ++j) cs = dot2(w[j], ones, cs);
        }
      }
      s1 += xor32(s1); s2 += xor32(s2); cs += xor32(cs);
      if (lane < 32) {
        const float mu = s1 * (1.f / 640.f);
        const float var = fmaxf(s2 * (1.f / 640.f) - mu * mu, 0.f);
        *(LAS f32x2*)(sm + L_STAT + (32 * lw + l31) * 8) = f32x2{mu, __builtin_amdgcn_rsqf(var + 1e-5f)};
        *(LAS float*)(sm + L_CS + (32 * lw + l31) * 4) = cs;
      }
      BAR();
      BAR();
    } else {
      const int wn = wid & 1, wt = wid >> 1;
      f32x16 acc[2][2] = {};
      const unsigned ax = L_P1 + (64 * wt + l31) * 128 + ((hi ^ ((l31 >> 1) & 7)) << 4);
      const unsigned aw = L_P1 + 16384 + (8 * hi + q4) * 256 + (((8 * wn + 2 * (G & 1) + (p4 & 1)) ^ (q4 << 2)) << 4) + 8 * (p4 >> 1);
      for (int s = 0; s < 10; ++s) {
        BAR();
        const unsigned so = (s & 3) * 32768;
#pragma unroll
        for (int ks = 0; ks < 4; ++ks) {
          bf16x8 xf[2], wf[2];
#pragma unroll
          for (int t = 0; t < 2; ++t) {
            xf[t] = ld_b128(sm + ((ax ^ (ks * 32)) + so + t * 4096));
            wf[t] = ld_tr2(sm + ((aw ^ (t * 64)) + so + ks * 4096), sm + ((aw ^ (t * 64)) + so + ks * 4096 + 1024));
          }
#pragma unroll
          for (int tj = 0; tj < 2; ++tj)
#pragma unroll
            for (int ti = 0; ti < 2; ++ti) acc[tj][ti] = MFMA32(wf[tj], xf[ti], acc[tj][ti]);
        }
      }
      STAMP(1);
      BAR();
      const float sc = (c < 5) ? QSCALE : 1.f;
#pragma unroll
      for (int ti = 0; ti < 2; ++ti) {
        const int tok = 64 * wt + 32 * ti + l31;
        const f32x2 st = *(LAS f32x2*)(sm + L_STAT + tok * 8);
        const float r = st[1] * sc, nm = -st[0] * r;
#pragma unroll
        for (int tj = 0; tj < 2; ++tj)
#pragma unroll
          for (int hf = 0; hf < 2; ++hf) {
            const int col = 64 * wn + 32 * tj + 16 * hf + 8 * hi;
            const f32x4 c0 = *(LAS f32x4*)(sm + L_CS + col * 4), c1 = *(LAS f32x4*)(sm + L_CS + col * 4 + 16);
            float v[8];
#pragma unroll
            for (int j = 0; j < 4; ++j) {
              v[j] = acc[tj][ti][8 * hf + j] * r + nm * c0[j];
              v[4 + j] = acc[tj][ti][8 * hf + 4 + j] * r + nm * c1[j];
            }
            u32x4 o = {pk2(v[0], v[1]), pk2(v[2], v[3]), pk2(v[4], v[5]), pk2(v[6], v[7])};
            ST128((o), (unsigned)(hi * 2048 + tok * 16), WS_QKV + (unsigned)(g * 240 + 16 * c + 8 * wn + 4 * tj + 2 * hf) * 2048);
          }
      }
      WAITV(0);
      BAR();
      if (tid == 0) st_flag(ws + WS_F1 + wgid * 128, epoch);
    }
  } else {
    const int cc = tid % 24, rg = tid / 24;
    float a[8] = {};
    if (rg < 20) {
      const char* src = ((cc < 12) ? wg : wu) + (size_t)rg * 3072 + g * 192 + (cc % 12) * 16;
      u32x4 v[32];
#pragma unroll
      for (int i = 0; i < 32; ++i) v[i] = *(const u32x4*)(src + (size_t)i * 20 * 3072);
#pragma unroll
      for (int i = 0; i < 32; ++i)
#pragma unroll
        for (int j = 0; j < 4; ++j) { a[2 * j] += bflo(v[i][j]); a[2 * j + 1] += bfhi(v[i][j]); }
      *(LAS f32x4*)(sm + L_P1 + (rg * 24 + cc) * 32) = f32x4{a[0], a[1], a[2], a[3]};
      *(LAS f32x4*)(sm + L_P1 + (rg * 24 + cc) * 32 + 16) = f32x4{a[4], a[5], a[6], a[7]};
    }
    BAR();
    if (tid < 48) {
      const int m = tid / 24, qd = tid % 24;
      f32x4 s = {};
      for (int r = 0; r < 20; ++r) s += *(LAS f32x4*)(sm + L_P1 + r * 768 + m * 384 + qd * 16);
      ST128((__builtin_bit_cast(u32x4, s)), (unsigned)((qd >> 2) * 128 + m * 64 + (qd & 3) * 16), WS_CSUM + (unsigned)g * 768);
      WAITV(0);
    }
    BAR();
    if (tid == 0) st_flag(ws + WS_F1 + wgid * 128, epoch);
  }
  STAMP(2);

  unsigned v3[5];
#pragma unroll
  for (int i = 0; i < 5; ++i) {
    const int u = 64 * i + lane, rho = u / 10, ch = u % 10;
    const int k = 8 * ((rho >> 2) & 3) + 4 * (rho >> 4) + (rho & 3);
    v3[i] = k * 1280 + ch * 16;
  }
  {
    if (tid < 128) *(LAS unsigned*)(sm + L_ONES + tid * 4) = 0x3f803f80u;
    if (wid == 0) {
      const int i = lane < 18 ? lane : 0;
      const int which = i / 6, po = which ? (i % 6) >> 1 : 0, e = i & 1;
      const int tile = (640 * which + 40 * c + 39 * e) >> 7;
      const int pn = g - po < 0 ? 0 : g - po;
      wait_flags(ws + WS_F1 + (pn * 16 + tile) * 128, epoch);
    }
    BAR();
    STAMP(3);
    const int hs = wid >> 2, qb = wid & 3;
    for (int pi = wid; pi < 60; pi += 8) {
      const int arr = pi / 30, j = (pi % 30) / 6, seg = pi % 6;
      const int pn = g - 2 + (seg >> 1);
      if (pn >= 0)
        glds_sc1(lane * 16, ws + WS_QKV + (size_t)(pn * 240 + 80 * (1 + arr) + 5 * c + j) * 2048 + (seg & 1) * 1024,
                 sm0 + (arr ? L_V + j * VSTR : L_K + j * 6144) + seg * 1024);
    }
    u32x4 q0 = __builtin_amdgcn_raw_buffer_load_b128(rs, (unsigned)(hi * 2048 + (32 * qb + l31) * 16), WS_QKV + (unsigned)(g * 240 + 5 * c + 3 * hs) * 2048, 16);
    u32x4 q1 = __builtin_amdgcn_raw_buffer_load_b128(rs, (unsigned)((32 * qb + l31) * 16), WS_QKV + (unsigned)(g * 240 + 5 * c + 2) * 2048, 16);
    if (hi || hs) { q1[0] = 0; q1[1] = 0; }
    if (hi || !hs) { q1[2] = 0; q1[3] = 0; }
    asm volatile("" : "+v"(q0), "+v"(q1));
    WAITV(0);
    BAR();
    STAMP(4);
    if (!isC) {
      for (int gr = 3 + (wid - 4); gr < 20; gr += 4) {
        const char* sb = wo + (size_t)gr * 32 * 1280 + (c & 7) * 160;
#pragma unroll
        for (int i = 0; i < 5; ++i) glds(v3[i], sb, sm0 + L_W3 + gr * 5120 + i * 1024);
      }
    }
    const bf16x8 qf0 = __builtin_bit_cast(bf16x8, q0), qf1 = __builtin_bit_cast(bf16x8, q1);
    const unsigned ak0 = L_K + (3 * hs + hi) * 6144 + l31 * 16, ak1 = L_K + 2 * 6144 + l31 * 16;
    const int kb_lo = (g >= 2) ? qb : (g == 1 ? 4 : 8), kb_hi = qb + 8;
    const float NINF = -__builtin_inff();
    f32x16 cD, cF;
#pragma unroll
    for (int r = 0; r < 16; ++r) {
      const int key = (r & 3) + 8 * (r >> 2) + 4 * hi;
      cD[r] = key <= l31 ? 0.f : NINF;
      cF[r] = key > l31 ? 0.f : NINF;
    }
    auto scores = [&](int kb, const f32x16& cin) {
      bf16x8 k0 = ld_b128(sm + ak0 + kb * 512), k1 = ld_b128(sm + ak1 + kb * 512);
      f32x16 s = MFMA32(k0, qf0, cin);
      return MFMA32(k1, qf1, s);
    };
    auto vmax = [&](const f32x16& s, float m) {
#pragma unroll
      for (int r = 0; r < 16; r += 2) m = fmaxf(fmaxf(m, s[r]), s[r + 1]);
      return m;
    };
    float m = vmax(scores(kb_hi, cD), NINF);
    int kb = kb_lo;
    if (kb_lo == qb) { m = vmax(scores(kb, cF), m); ++kb; }
    const f32x16 zero16 = {};
    for (; kb < kb_hi; ++kb) m = vmax(scores(kb, zero16), m);
    m = fmaxf(m, xor32(m));
    f32x16 cM;
#pragma unroll
    for (int r = 0; r < 16; ++r) { cM[r] = -m; cD[r] -= m; cF[r] -= m; }
    const bool isOne = (G & 1) && p4;
    const unsigned av0 = isOne ? L_ONES
                               : L_V + (4 * hi + q4) * 16 + ((G & 1) ? 2 * VSTR + 8 * hs : (3 * hs + (p4 & 1)) * VSTR + 8 * (p4 >> 1));
    const unsigned avs = isOne ? 0 : 512;
    f32x16 o = {};
    auto pv = [&](int kb_, const f32x16& cin) {
      f32x16 s = scores(kb_, cin);
      unsigned pw[8];
#pragma unroll
      for (int r = 0; r < 8; ++r) pw[r] = pk2(__builtin_amdgcn_exp2f(s[2 * r]), __builtin_amdgcn_exp2f(s[2 * r + 1]));
      const unsigned av = av0 + kb_ * avs;
      bf16x8 vf0 = ld_tr2(sm + av, sm + av + 128), vf1 = ld_tr2(sm + av + 256, sm + av + 384);
      o = MFMA32(vf0, __builtin_bit_cast(bf16x8, (u32x4{pw[0], pw[1], pw[2], pw[3]})), o);
      o = MFMA32(vf1, __builtin_bit_cast(bf16x8, (u32x4{pw[4], pw[5], pw[6], pw[7]})), o);
    };
    pv(kb_hi, cD);
    kb = kb_lo;
    if (kb_lo == qb) { pv(kb, cF); ++kb; }
    for (; kb < kb_hi; ++kb) pv(kb, cM);
    auto sw = __builtin_amdgcn_permlane32_swap(__float_as_uint(o[8]), __float_as_uint(o[8]), false, false);
    const float rl = __builtin_amdgcn_rcpf(__uint_as_float(sw[1]));
    const int row = 32 * qb + l31;
    *(LAS u32x4*)(sm + L_OST + (3 * hs + hi) * 2048 + row * 16) =
        u32x4{pk2(o[0] * rl, o[1] * rl), pk2(o[2] * rl, o[3] * rl), pk2(o[4] * rl, o[5] * rl), pk2(o[6] * rl, o[7] * rl)};
    if (!hi) *(LAS u32x2*)(sm + L_OST + 2 * 2048 + row * 16 + 8 * hs) = u32x2{pk2(o[8] * rl, o[9] * rl), pk2(o[10] * rl, o[11] * rl)};
    STAMP(5);
    BAR();
  }

  if (!isC) {
    const int lw = wid - 4;
    unsigned v4[3];
#pragma unroll
    for (int i = 0; i < 3; ++i) { const int u = 64 * i + lane; v4[i] = (u / 12) * 3072 + (u % 12) * 16; }
    const char* w4b = ((lw >> 1) ? wu : wg) + (size_t)(lw & 1) * 32 * 3072 + c * 192;
    const char* w5b = wd + (size_t)lw * 32 * 1280 + (c & 7) * 160;
    auto issue = [&](int st) {
      const unsigned slot = sm0 + L_RING + (st % 6) * RSLOT;
      if (st < 10) {
#pragma unroll
        for (int i = 0; i < 6; ++i)
          glds(v4[i % 3], w4b + (size_t)(64 * st + 16 * (i / 3)) * 3072, slot + (lw >> 1) * UPOFF + (lw & 1) * 6144 + i * 1024);
      } else {
#pragma unroll
        for (int i = 0; i < 5; ++i) glds(v3[i], w5b + (size_t)(st - 10) * 128 * 1280, slot + lw * 5120 + i * 1024);
      }
    };
    if (lw < 3) {
      const char* sb = wo + (size_t)lw * 32 * 1280 + (c & 7) * 160;
#pragma unroll
      for (int i = 0; i < 5; ++i) glds(v3[i], sb, sm0 + L_W3 + lw * 5120 + i * 1024);
    }
    issue(0); issue(1);
    BAR();
    WAITV(12);
    BAR();
    BAR();
    issue(2); issue(3); issue(4); issue(5);
    BAR();
#pragma unroll
    for (int s = 0; s < 22; ++s) {
      int last = s + 4 < 5 ? 5 : s + 4; if (s == 10) last = 15; if (last > 21) last = 21;
      int n = 0;
      for (int t = s + 2; t <= last; ++t) n += (t < 10) ? 6 : 5;
      switch (n) {
        case 24: WAITV(24); break; case 18: WAITV(18); break; case 17: WAITV(17); break; case 16: WAITV(16); break;
        case 15: WAITV(15); break; case 20: WAITV(20); break; case 10: WAITV(10); break; case 5: WAITV(5); break;
        default: WAITV(0); break;
      }
      BAR();
      if (s >= 1 && s != 10 && s + 5 <= 21) issue(s + 5);
      if (s == 9) {
        BAR();
        issue(15);
        BAR();
      }
    }
    if (b == 0 && lw == 3) {
      const u64 ep = mix64(ld_flag(ws + WS_EPOCH));
      for (int i = 0; i < 4; ++i) wait_flags(ws + WS_F2 + (i * 64 + lane) * 128, ep);
      if (lane == 0) st_flag(ws + WS_EPOCH, ep);
    }
    return;
  }

  for (int pi = wid; pi < 10; pi += 4) {
    u32x4 v = *(LAS u32x4*)(sm + L_OST + pi * 1024 + lane * 16);
    ST128((v), (unsigned)(lane * 16), WS_O + (unsigned)(g * 80 + 5 * c) * 2048 + pi * 1024);
  }
  WAITV(0);
  BAR();
  if (tid == 0) st_flag(ws + WS_F2 + wgid * 128, epoch);
  STAMP(6);

  const int r3 = 64 * (c >> 3) + 16 * wid + l15;
  const unsigned aw3 = (4 * G + q4) * 160 + 8 * p4;
  unsigned hpk[5][2];
  {
    u32x2 xr[5];
#pragma unroll
    for (int tt = 0; tt < 5; ++tt) xr[tt] = *(const u32x2*)(x + (size_t)(128 * g + r3) * 1280 + (c & 7) * 160 + tt * 32 + G * 8);
    wait_flags(ws + WS_F2 + (g * 16 + l15) * 128, epoch);
    STAMP(7);
    u32x4 xf[20];
#pragma unroll
    for (int s = 0; s < 20; ++s)
      xf[s] = __builtin_amdgcn_raw_buffer_load_b128(rs, (unsigned)(G * 2048 + r3 * 16), WS_O + (unsigned)(g * 80 + 4 * s) * 2048, 16);
    BAR();
    f32x4 acc[5] = {};
    {
      bf16x8 wf[3][5];
      unsigned ab[2] = {L_W3 + aw3, L_W3 + aw3 + 10 * 5120};
      OPAQUE(ab[0]); OPAQUE(ab[1]);
      auto rd = [&](int i) {
#pragma unroll
        for (int tt = 0; tt < 5; ++tt) {
          LAS char* p = sm + ab[i / 10] + (i % 10) * 5120 + tt * 32;
          wf[i % 3][tt] = ld_tr2(p, p + 2560);
        }
      };
      rd(0); rd(1);
      __builtin_amdgcn_sched_barrier(0);
#pragma unroll
      for (int i = 0; i < 20; ++i) {
        if (i + 2 < 20) rd(i + 2);
#pragma unroll
        for (int tt = 0; tt < 5; ++tt) acc[tt] = MFMA16(wf[i % 3][tt], __builtin_bit_cast(bf16x8, xf[i]), acc[tt]);
#pragma unroll
        for (int tt = 0; tt < 5; ++tt) { SGB(0x8, 1); SGB(0x100, 2); }
        __builtin_amdgcn_sched_barrier(0);
      }
    }
    BAR();
    STAMP(8);
    float s1 = 0.f, s2 = 0.f;
#pragma unroll
    for (int tt = 0; tt < 5; ++tt)
#pragma unroll
      for (int d = 0; d < 2; ++d) {
        const unsigned a = pk2(acc[tt][2 * d], acc[tt][2 * d + 1]);
        const unsigned h = pk2(bflo(a) + bflo(xr[tt][d]), bfhi(a) + bfhi(xr[tt][d]));
        hpk[tt][d] = h;
        s1 += bflo(h) + bfhi(h);
        s2 += bflo(h) * bflo(h) + bfhi(h) * bfhi(h);
      }
    s1 += __shfl_xor(s1, 16); s2 += __shfl_xor(s2, 16);
    s1 += xor32(s1); s2 += xor32(s2);
    const unsigned hb = WS_H + (unsigned)(g * 80 + 10 * (c & 7)) * 2048;
#pragma unroll
    for (int pr = 0; pr < 2; ++pr) {
      auto a0 = __builtin_amdgcn_permlane16_swap(hpk[2 * pr][0], hpk[2 * pr + 1][0], false, false);
      auto a1 = __builtin_amdgcn_permlane16_swap(hpk[2 * pr][1], hpk[2 * pr + 1][1], false, false);
      ST128((u32x4{a0[0], a1[0], a0[1], a1[1]}), (unsigned)((2 * (G & 1) + (G >> 1)) * 2048 + r3 * 16), hb + pr * 4 * 2048);
    }
    {
      auto a0 = __builtin_amdgcn_permlane16_swap(hpk[4][0], hpk[4][0], false, false);
      auto a1 = __builtin_amdgcn_permlane16_swap(hpk[4][1], hpk[4][1], false, false);
      if (!(G & 1))
        ST128((u32x4{a0[0], a1[0], a0[1], a1[1]}), (unsigned)((G >> 1) * 2048 + r3 * 16), hb + 8 * 2048);
    }
    if (G == 0)
      ST64((u32x2{__float_as_uint(s1), __float_as_uint(s2)}), (unsigned)(r3 * 8), WS_STAT + (unsigned)(g * 8 + (c & 7)) * 1024);
    WAITV(0);
    BAR();
    if (tid == 0) st_flag(ws + WS_F3 + wgid * 128, epoch);
    STAMP(9);
  }

  {
    wait_flags(lane < 16 ? ws + WS_F3 + (g * 16 + lane) * 128 : ws + WS_F1 + (c * 16 + 15) * 128, epoch);
    STAMP(10);
    const int r4 = 32 * wid + l31;
    const unsigned vx = hi * 2048 + r4 * 16;
    const unsigned hb = WS_H + (unsigned)(g * 80) * 2048;
    constexpr int WIN = 12;
    u32x4 xf[WIN];
#pragma unroll
    for (int i = 0; i < WIN; ++i) xf[i] = __builtin_amdgcn_raw_buffer_load_b128(rs, vx, hb + i * 4096, 16);
    u32x2 stp[4];
#pragma unroll
    for (int i = 0; i < 4; ++i) stp[i] = __builtin_amdgcn_raw_buffer_load_b64(rs, (unsigned)(hi * 4096 + r4 * 8), WS_STAT + (unsigned)(g * 8 + i) * 1024, 16);
    if (wid == 0 && lane < 48) {
      u32x4 v = __builtin_amdgcn_raw_buffer_load_b128(rs, (unsigned)(lane * 16), WS_CSUM + (unsigned)c * 768, 16);
      *(LAS u32x4*)(sm + L_CSUM4 + lane * 16) = v;
    }
    f32x16 acc[6] = {};
    const unsigned aw = L_RING + (G & 1) * UPOFF + (8 * hi + q4) * 192 + 16 * (p4 & 1) + 8 * (p4 >> 1);
    {
      bf16x8 wf[2][6];
      unsigned so = aw;
      auto rd = [&](int i) {
        if (i % 4 == 0) { so = aw + ((i / 4) % 6) * RSLOT; OPAQUE(so); }
#pragma unroll
        for (int tt = 0; tt < 6; ++tt) {
          LAS char* p = sm + so + (i % 4) * 3072 + tt * 32;
          wf[i & 1][tt] = ld_tr2(p, p + 768);
        }
      };
      BAR();
      rd(0);
      __builtin_amdgcn_sched_barrier(0);
#pragma unroll
      for (int i = 0; i < 40; ++i) {
        if (i % 4 == 0 && i > 0) RAWBAR();
        if (i + 1 < 40) rd(i + 1);
        const bf16x8 xv = __builtin_bit_cast(bf16x8, xf[i % WIN]);
#pragma unroll
        for (int tt = 0; tt < 6; ++tt) acc[tt] = MFMA32(wf[i & 1][tt], xv, acc[tt]);
        if (i + WIN < 40) xf[i % WIN] = __builtin_amdgcn_raw_buffer_load_b128(rs, vx, hb + (i + WIN) * 4096, 16);
#pragma unroll
        for (int tt = 0; tt < 6; ++tt) { SGB(0x8, 1); SGB(0x100, 2); }
        __builtin_amdgcn_sched_barrier(0);
      }
    }
    BAR();
    STAMP(11);
    float s1 = 0.f, s2 = 0.f;
#pragma unroll
    for (int i = 0; i < 4; ++i) { s1 += __uint_as_float(stp[i][0]); s2 += __uint_as_float(stp[i][1]); }
    s1 += xor32(s1); s2 += xor32(s2);
    const float mu = s1 * (1.f / 640.f);
    const float r = __builtin_amdgcn_rsqf(fmaxf(s2 * (1.f / 640.f) - mu * mu, 0.f) + 1e-5f), nm = -mu * r;
#pragma unroll
    for (int tt = 0; tt < 6; ++tt) {
      LAS char* cp = sm + L_CSUM4 + tt * 128 + hi * 32;
      const f32x4 g0 = *(LAS f32x4*)cp, g1 = *(LAS f32x4*)(cp + 16), u0 = *(LAS f32x4*)(cp + 64), u1 = *(LAS f32x4*)(cp + 80);
      unsigned o[4];
#pragma unroll
      for (int d = 0; d < 4; ++d) {
        const f32x4 gc = d < 2 ? g0 : g1, uc = d < 2 ? u0 : u1;
        const int e = (2 * d) & 3;
        const unsigned gp = pk2(acc[tt][2 * d] * r + nm * gc[e], acc[tt][2 * d + 1] * r + nm * gc[e + 1]);
        const unsigned up = pk2(acc[tt][8 + 2 * d] * r + nm * uc[e], acc[tt][8 + 2 * d + 1] * r + nm * uc[e + 1]);
        o[d] = pk2(bflo(gp) * bflo(up), bfhi(gp) * bfhi(up));
      }
      ST128((u32x4{o[0], o[1], o[2], o[3]}), vx, WS_HH + (unsigned)(g * 192 + 12 * c + 2 * tt) * 2048);
    }
    WAITV(0);
    BAR();
    if (tid == 0) st_flag(ws + WS_F4 + wgid * 128, epoch);
    STAMP(12);
  }

  {
    wait_flags(ws + WS_F4 + (g * 16 + l15) * 128, epoch);
    STAMP(13);
    const unsigned vx = G * 2048 + r3 * 16;
    const unsigned hb = WS_HH + (unsigned)(g * 192) * 2048;
    constexpr int WIN = 32;
    u32x4 xf[WIN];
#pragma unroll
    for (int i = 0; i < WIN; ++i) xf[i] = __builtin_amdgcn_raw_buffer_load_b128(rs, vx, hb + i * 8192, 16);
    f32x4 acc[5] = {};
    {
      bf16x8 wf[3][5];
      unsigned so = 0;
      auto rd = [&](int i) {
        if (i % 4 == 0) { so = L_RING + aw3 + ((i / 4 + 10) % 6) * RSLOT; OPAQUE(so); }
#pragma unroll
        for (int tt = 0; tt < 5; ++tt) {
          LAS char* p = sm + so + (i % 4) * 5120 + tt * 32;
          wf[i % 3][tt] = ld_tr2(p, p + 2560);
        }
      };
      BAR();
      rd(0); rd(1);
      __builtin_amdgcn_sched_barrier(0);
#pragma unroll
      for (int i = 0; i < 48; ++i) {
        if (i % 4 == 0 && i > 0) RAWBAR();
        if (i + 2 < 48) rd(i + 2);
        const bf16x8 xv = __builtin_bit_cast(bf16x8, xf[i % WIN]);
#pragma unroll
        for (int tt = 0; tt < 5; ++tt) acc[tt] = MFMA16(wf[i % 3][tt], xv, acc[tt]);
        if (i + WIN < 48) xf[i % WIN] = __builtin_amdgcn_raw_buffer_load_b128(rs, vx, hb + (i + WIN) * 8192, 16);
#pragma unroll
        for (int tt = 0; tt < 5; ++tt) { SGB(0x8, 1); SGB(0x100, 2); }
        __builtin_amdgcn_sched_barrier(0);
      }
    }
    STAMP(14);
    unsigned opk[5][2];
#pragma unroll
    for (int tt = 0; tt < 5; ++tt)
#pragma unroll
      for (int d = 0; d < 2; ++d) {
        const unsigned a = pk2(acc[tt][2 * d], acc[tt][2 * d + 1]);
        opk[tt][d] = pk2(bflo(a) + bflo(hpk[tt][d]), bfhi(a) + bfhi(hpk[tt][d]));
      }
    char* op = out + (size_t)(128 * g + r3) * 1280 + (c & 7) * 160;
#pragma unroll
    for (int pr = 0; pr < 2; ++pr) {
      auto a0 = __builtin_amdgcn_permlane16_swap(opk[2 * pr][0], opk[2 * pr + 1][0], false, false);
      auto a1 = __builtin_amdgcn_permlane16_swap(opk[2 * pr][1], opk[2 * pr + 1][1], false, false);
      *(u32x4*)(op + pr * 64 + (2 * (G & 1) + (G >> 1)) * 16) = u32x4{a0[0], a1[0], a0[1], a1[1]};
    }
    *(u32x2*)(op + 128 + G * 8) = u32x2{opk[4][0], opk[4][1]};
    STAMP(15);
  }
}

extern "C" void kernel_launch(void* const* d_in, const int* in_sizes, int n_in, void* d_out, int out_size, void* d_ws, size_t ws_size,
                              hipStream_t stream) {
  fused_block<<<256, 512, 0, stream>>>((const char*)d_in[0], (const char*)d_in[1], (const char*)d_in[2], (const char*)d_in[4],
                                       (const char*)d_in[5], (const char*)d_in[6], (char*)d_out, (char*)d_ws);
}
```

```cpp
#include <hip/hip_runtime.h>
#include <stdint.h>

typedef unsigned long long u64;
typedef __attribute__((ext_vector_type(8))) short bf16x8;
typedef __attribute__((ext_vector_type(4))) short s16x4;
typedef __attribute__((ext_vector_type(2))) __bf16 bf16x2;
typedef __attribute__((ext_vector_type(16))) float f32x16;
typedef __attribute__((ext_vector_type(4))) float f32x4;
typedef __attribute__((ext_vector_type(2))) float f32x2;
typedef __attribute__((ext_vector_type(4))) unsigned u32x4;
typedef __attribute__((ext_vector_type(2))) unsigned u32x2;
#define LAS __attribute__((address_space(3)))
#define DI __device__ __forceinline__

constexpr unsigned WS_EPOCH = 0;
constexpr unsigned FL = 256 * 128;
constexpr unsigned WS_F0 = 4096, WS_F1 = WS_F0 + FL, WS_F2 = WS_F1 + FL, WS_F3 = WS_F2 + FL, WS_F4 = WS_F3 + FL;
constexpr unsigned WS_F1P = WS_F4 + 3 * FL;
constexpr unsigned WS_CSUM = WS_F1P + 2048;
constexpr unsigned WS_ST1 = WS_CSUM + 16 * 768;
constexpr unsigned WS_CS1 = WS_ST1 + 2048 * 8;
constexpr unsigned WS_STAT = WS_CS1 + 16 * 16 * 512;
constexpr unsigned WS_QKV = 1u << 20;
constexpr unsigned WS_O = WS_QKV + 16 * 240 * 2048;
constexpr unsigned WS_H = WS_O + 16 * 80 * 2048;
constexpr unsigned WS_HH = WS_H + 16 * 80 * 2048;
constexpr unsigned WS_W3 = WS_HH + 16 * 192 * 2048;
constexpr unsigned WS_W4 = WS_W3 + 8 * 100 * 1024;
constexpr unsigned WS_W5 = WS_W4 + 16 * 240 * 1024;
constexpr unsigned WS_END = WS_W5 + 8 * 240 * 1024;
constexpr unsigned WS_DIAG = 32u << 20;
static_assert(WS_STAT + 16 * 8 * 1024 <= WS_QKV && WS_END <= WS_DIAG, "workspace map");

constexpr unsigned L_STAT = 0, L_CS = 1024;
constexpr unsigned L_W = 1536;
constexpr unsigned L_ONES = 1792;
constexpr unsigned L_KT = 2304;
constexpr unsigned L_RP = 2560;
constexpr unsigned L_OST = 2560;
constexpr unsigned L_JUNK = 31488;
constexpr unsigned L_HSUM = 131072;
constexpr unsigned L_CSUM4 = 0, L_ST4 = 12800;
constexpr int CODE_LINES = 48;
constexpr unsigned L_P1 = 32768;
constexpr unsigned L_K = 12800, L_V = L_K + 5 * 6144, VSTR = 6208;
constexpr unsigned L_RING = 20480, RSLOT = 20480;
constexpr unsigned slot_of(int i) { return L_RING + ((i + 3) % 7) * RSLOT; }
static_assert(L_V + 5 * VSTR <= L_RING + 3 * RSLOT, "LDS map");
enum { W_LAND1 = 0, W_LANDW = 8, W_CONS1 = 16, W_CONSW = 32, W_GO = 48, W_S1DONE = 52, W_STATS = 56, W_WREADY = 60, W_RDY = 64, W_DONE = 80, W_CNT = 96, W_INIT = 128 };

#define P3REL 2
constexpr float QSCALE = 0.22360679774997896f * 1.4426950408889634f;

#define MFMA32(a, b, c) __builtin_amdgcn_mfma_f32_32x32x16_bf16((a), (b), (c), 0, 0, 0)
#define MFMA16(a, b, c) __builtin_amdgcn_mfma_f32_16x16x32_bf16((a), (b), (c), 0, 0, 0)
#define BAR() asm volatile("s_waitcnt lgkmcnt(0)\n\ts_barrier" ::: "memory")
#define SCHED() __builtin_amdgcn_sched_barrier(0)
#define RAWBAR() do { SCHED(); asm volatile("s_barrier" ::: "memory"); SCHED(); } while (0)
#define OPAQUE(v) asm volatile("" : "+v"(v))
#define SGB(m, n) __builtin_amdgcn_sched_group_barrier((m), (n), 0)
#define WAITV(n) asm volatile("s_waitcnt vmcnt(%0)" ::"i"(n) : "memory")

#define STAMP(i) do {} while (0)
#define STAMP2(i) do {} while (0)
#define STAMP_DUMP() do {} while (0)

DI void glds(unsigned voff, const char* sbase, unsigned ldst) {
  unsigned sv;
  asm volatile("s_mov_b32 %0, m0\n\ts_mov_b32 m0, %3\n\ts_nop 2\n\tglobal_load_lds_dwordx4 %1, %2\n\ts_mov_b32 m0, %0"
               : "=&s"(sv) : "v"(voff), "s"(sbase), "s"(ldst) : "memory");
}
DI void glds_sc1(unsigned voff, const char* sbase, unsigned ldst) {
  unsigned sv;
  asm volatile("s_mov_b32 %0, m0\n\ts_mov_b32 m0, %3\n\ts_nop 2\n\tglobal_load_lds_dwordx4 %1, %2 sc1\n\ts_mov_b32 m0, %0"
               : "=&s"(sv) : "v"(voff), "s"(sbase), "s"(ldst) : "memory");
}
DI void glds5_sc1(unsigned voff, const char* sbase, unsigned ldst) {
  unsigned sv;
  asm volatile("s_mov_b32 %0, m0\n\ts_mov_b32 m0, %3\n\ts_nop 2\n\t"
               "global_load_lds_dwordx4 %1, %2 offset:-2048 sc1\n\tglobal_load_lds_dwordx4 %1, %2 offset:-1024 sc1\n\t"
               "global_load_lds_dwordx4 %1, %2 sc1\n\tglobal_load_lds_dwordx4 %1, %2 offset:1024 sc1\n\t"
               "global_load_lds_dwordx4 %1, %2 offset:2048 sc1\n\ts_mov_b32 m0, %0"
               : "=&s"(sv) : "v"(voff), "s"(sbase + 2048), "s"(ldst + 2048) : "memory");
}
DI void glds5(unsigned voff, const char* sbase, unsigned ldst) {
  unsigned sv;
  asm volatile("s_mov_b32 %0, m0\n\ts_mov_b32 m0, %3\n\ts_nop 2\n\t"
               "global_load_lds_dwordx4 %1, %2 offset:-2048\n\tglobal_load_lds_dwordx4 %1, %2 offset:-1024\n\t"
               "global_load_lds_dwordx4 %1, %2\n\tglobal_load_lds_dwordx4 %1, %2 offset:1024\n\t"
               "global_load_lds_dwordx4 %1, %2 offset:2048\n\ts_mov_b32 m0, %0"
               : "=&s"(sv) : "v"(voff), "s"(sbase + 2048), "s"(ldst + 2048) : "memory");
}
DI void glds8(const unsigned (&v)[8], const char* sbase, unsigned ldst) {
  unsigned sv;
  asm volatile("s_mov_b32 %0, m0\n\ts_mov_b32 m0, %10\n\ts_nop 2\n\t"
               "global_load_lds_dwordx4 %1, %9 offset:-4096\n\tglobal_load_lds_dwordx4 %2, %9 offset:-3072\n\t"
               "global_load_lds_dwordx4 %3, %9 offset:-2048\n\tglobal_load_lds_dwordx4 %4, %9 offset:-1024\n\t"
               "global_load_lds_dwordx4 %5, %9\n\tglobal_load_lds_dwordx4 %6, %9 offset:1024\n\t"
               "global_load_lds_dwordx4 %7, %9 offset:2048\n\tglobal_load_lds_dwordx4 %8, %9 offset:3072\n\ts_mov_b32 m0, %0"
               : "=&s"(sv) : "v"(v[0]), "v"(v[1]), "v"(v[2]), "v"(v[3]), "v"(v[4]), "v"(v[5]), "v"(v[6]), "v"(v[7]),
                 "s"(sbase), "s"(ldst + 4096) : "memory");
}
DI void glds10_sc1(unsigned voff, const char* sbase, unsigned ldst) {
  unsigned sv;
  asm volatile("s_mov_b32 %0, m0\n\ts_mov_b32 m0, %4\n\ts_nop 2\n\t"
               "global_load_lds_dwordx4 %1, %3 offset:-4096 sc1\n\tglobal_load_lds_dwordx4 %1, %3 offset:-3072 sc1\n\t"
               "global_load_lds_dwordx4 %1, %3 offset:-2048 sc1\n\tglobal_load_lds_dwordx4 %1, %3 offset:-1024 sc1\n\t"
               "global_load_lds_dwordx4 %1, %3 sc1\n\tglobal_load_lds_dwordx4 %1, %3 offset:1024 sc1\n\t"
               "global_load_lds_dwordx4 %1, %3 offset:2048 sc1\n\tglobal_load_lds_dwordx4 %1, %3 offset:3072 sc1\n\t"
               "s_add_u32 m0, m0, 0x1000\n\ts_nop 0\n\t"
               "global_load_lds_dwordx4 %2, %3 sc1\n\tglobal_load_lds_dwordx4 %2, %3 offset:1024 sc1\n\ts_mov_b32 m0, %0"
               : "=&s"(sv) : "v"(voff), "v"(voff + 4096), "s"(sbase + 4096), "s"(ldst + 4096) : "memory", "scc");
}
DI void glds2_sc1(unsigned voff, const char* sbase, unsigned ldst) {
  unsigned sv;
  asm volatile("s_mov_b32 %0, m0\n\ts_mov_b32 m0, %3\n\ts_nop 2\n\t"
               "global_load_lds_dwordx4 %1, %2 sc1\n\tglobal_load_lds_dwordx4 %1, %2 offset:1024 sc1\n\ts_mov_b32 m0, %0"
               : "=&s"(sv) : "v"(voff), "s"(sbase), "s"(ldst) : "memory");
}
#define ST128(v, voff, soff) __builtin_amdgcn_raw_buffer_store_b128((v), rs, (unsigned)(voff) + (unsigned)(soff), 0, 16)
#define ST64(v, voff, soff) __builtin_amdgcn_raw_buffer_store_b64((v), rs, (unsigned)(voff) + (unsigned)(soff), 0, 16)
#define LD128(voff, soff) __builtin_amdgcn_raw_buffer_load_b128(rs, (unsigned)(voff), (unsigned)(soff), 16)
#define LD64(voff, soff) __builtin_amdgcn_raw_buffer_load_b64(rs, (unsigned)(voff), (unsigned)(soff), 16)
DI bf16x8 ld_b128(LAS char* p) { return *(LAS bf16x8*)p; }
DI s16x4 ld_tr(LAS char* p) { return __builtin_amdgcn_ds_read_tr16_b64_v4i16((LAS s16x4*)p); }
DI bf16x8 ld_tr2(LAS char* p0, LAS char* p1) {
  s16x4 a = ld_tr(p0), b = ld_tr(p1);
  return __builtin_shufflevector(a, b, 0, 1, 2, 3, 4, 5, 6, 7);
}
DI unsigned pk2(float lo, float hi) {
  f32x2 v = {lo, hi};
  return __builtin_bit_cast(unsigned, __builtin_convertvector(v, bf16x2));
}
DI float bflo(unsigned w) { return __uint_as_float(w << 16); }
DI float bfhi(unsigned w) { return __uint_as_float(w & 0xffff0000u); }
DI float sum32(float v) {
  auto r = __builtin_amdgcn_permlane32_swap(__float_as_uint(v), __float_as_uint(v), false, false);
  return __uint_as_float(r[0]) + __uint_as_float(r[1]);
}
DI float sum16(float v) {
  auto r = __builtin_amdgcn_permlane16_swap(__float_as_uint(v), __float_as_uint(v), false, false);
  return __uint_as_float(r[0]) + __uint_as_float(r[1]);
}
DI float sum8(float v) {
  v += __uint_as_float(__builtin_amdgcn_update_dpp(0, __float_as_uint(v), 0xB1, 0xf, 0xf, true));
  v += __uint_as_float(__builtin_amdgcn_update_dpp(0, __float_as_uint(v), 0x4E, 0xf, 0xf, true));
  v += __uint_as_float(__builtin_amdgcn_update_dpp(0, __float_as_uint(v), 0x141, 0xf, 0xf, true));
  return v;
}
DI float max32(float v) {
  auto r = __builtin_amdgcn_permlane32_swap(__float_as_uint(v), __float_as_uint(v), false, false);
  return fmaxf(__uint_as_float(r[0]), __uint_as_float(r[1]));
}
DI u64 mix64(u64 z) {
  z += 0x9E3779B97F4A7C15ull; z = (z ^ (z >> 30)) * 0xBF58476D1CE4E5B9ull; z = (z ^ (z >> 27)) * 0x94D049BB133111EBull;
  return z ^ (z >> 31);
}
DI u64 ld_flag(const char* p) { return __hip_atomic_load((const u64*)p, __ATOMIC_RELAXED, __HIP_MEMORY_SCOPE_AGENT); }
DI void st_flag(char* p, u64 v) { __hip_atomic_store((u64*)p, v, __ATOMIC_RELAXED, __HIP_MEMORY_SCOPE_AGENT); }
DI void wait_flags_from(const char* p, u64 v, u64 epoch) {
  for (unsigned spins = 0; spins < (1u << 18); ++spins) {
    if (__all(v == epoch)) break;
    __builtin_amdgcn_s_sleep(1);
    v = ld_flag(p);
  }
  asm volatile("" ::: "memory");
}
DI void wait_flags(const char* p, u64 epoch) { wait_flags_from(p, ld_flag(p), epoch); }
DI f32x16 mfma32_c(bf16x8 a, bf16x8 b, const f32x16& c) {
  f32x16 d;
  asm volatile("s_nop 1\n\tv_mfma_f32_32x32x16_bf16 %0, %1, %2, %3" : "=&v"(d) : "v"(a), "v"(b), "v"(c));
  return d;
}

DI float dot2(unsigned a, unsigned b, float c) {
  return __builtin_amdgcn_fdot2_f32_bf16(__builtin_bit_cast(bf16x2, a), __builtin_bit_cast(bf16x2, b), c, false);
}
DI int rfl(int v) { return __builtin_amdgcn_readfirstlane(v); }
DI int mini(int a, int b) { return a < b ? a : b; }
typedef __attribute__((ext_vector_type(2))) int i32x2;
typedef __attribute__((ext_vector_type(4))) int i32x4;
#define FENCE() asm volatile("" ::: "memory")
constexpr int SPIN_CAP = 1 << 22;
DI void lds_wait_eq(unsigned v, unsigned addr, unsigned want) {
  unsigned s0, cnt;
  asm volatile("s_mov_b32 %2, 0x400000\n"
               "1:\n\tv_readfirstlane_b32 %1, %0\n\ts_cmp_eq_u32 %1, %4\n\ts_cbranch_scc1 2f\n\t"
               "ds_read_b32 %0, %3\n\ts_sub_u32 %2, %2, 1\n\ts_waitcnt lgkmcnt(0)\n\ts_cbranch_scc0 1b\n"
               "2:"
               : "+v"(v), "=&s"(s0), "=&s"(cnt) : "v"(addr), "s"(want) : "memory", "scc");
}
DI void lds_wait_ge(unsigned addr, unsigned need) {
  unsigned v, s0, cnt;
  asm volatile("s_mov_b32 %2, 0x400000\n"
               "1:\n\tds_read_b32 %0, %3\n\ts_waitcnt lgkmcnt(0)\n\tv_readfirstlane_b32 %1, %0\n\ts_cmp_ge_u32 %1, %4\n\ts_cbranch_scc1 2f\n\t"
               "s_sub_u32 %2, %2, 1\n\ts_cbranch_scc0 1b\n"
               "2:"
               : "=&v"(v), "=&s"(s0), "=&s"(cnt) : "v"(addr), "s"(need) : "memory", "scc");
}
DI void lds_wait_halves(unsigned v, unsigned addr, unsigned need) {
  unsigned s0, s1, cnt;
  asm volatile("s_mov_b32 %3, 0x400000\n"
               "1:\n\tv_readfirstlane_b32 %1, %0\n\ts_lshr_b32 %2, %1, 16\n\ts_and_b32 %1, %1, 0xffff\n\ts_min_u32 %1, %1, %2\n\t"
               "s_cmp_ge_u32 %1, %5\n\ts_cbranch_scc1 2f\n\t"
               "ds_read_b32 %0, %4\n\ts_sub_u32 %3, %3, 1\n\ts_waitcnt lgkmcnt(0)\n\ts_cbranch_scc0 1b\n"
               "2:"
               : "+v"(v), "=&s"(s0), "=&s"(s1), "=&s"(cnt) : "v"(addr), "s"(need) : "memory", "scc");
}

__global__ __launch_bounds__(512, 2) void fused_block(const char* __restrict__ x, const char* __restrict__ wqkv, const char* __restrict__ wo,
                                                      const char* __restrict__ wg, const char* __restrict__ wu, const char* __restrict__ wd,
                                                      char* __restrict__ out, char* ws) {
  __shared__ __attribute__((aligned(16))) char smem_[163840];
  LAS char* sm = (LAS char*)smem_;
  const unsigned sm0 = (unsigned)(uintptr_t)sm;
  const int tid = threadIdx.x, lane = tid & 63;
  const int wid = __builtin_amdgcn_readfirstlane(tid >> 6);
  STAMP(0);
  const int b = blockIdx.x, g = 4 * ((b & 7) >> 1) + (b >> 6), c = 8 * (b & 1) + ((b >> 3) & 7);
  const int wgid = g * 16 + c;
  const int c1 = ((g & 1) && (c & 7) == 7) ? c ^ 8 : c;
  const bool lx = !(wid & 1);
  unsigned vo[8];
  {
    const unsigned rsh = lx ? 3 : 4, rstr = lx ? 1280 : 3840, pstr = (lx ? 8 * 1280 : 4 * 3840) - 1024;
    const unsigned cm = lx ? 7 : 15, csh = lx ? 0 : 2, chx = lx ? 64 : 0;
    const unsigned ch0 = (lane & cm) ^ ((lane >> 4) << csh);
    const unsigned vb = (lane >> rsh) * rstr + 4096;
#pragma unroll
    for (int i = 0; i < 8; ++i) vo[i] = vb + i * pstr + ((ch0 << 4) ^ ((i & 1) ? chx : 0));
  }
  const char* const rsb = (lx ? x : wqkv) + (lx ? g * (128 * 1280) : c1 * 256);
  const unsigned hstep = lx ? 64 * 1280 : 32 * 3840, sstep = lx ? 128 : 64 * 3840, rdst = sm0 + L_P1 + (wid & 1) * 16384;
  if (c1 < 15 && wid < 4) {
#pragma unroll
    for (int s = 0; s < 2; ++s) glds8(vo, rsb + s * sstep + (wid >> 1) * hstep, rdst + s * 32768 + (wid >> 1) * 8192);
  }
  SCHED();
  const auto rs = __builtin_amdgcn_make_buffer_rsrc(ws, 0, WS_END, 0x00020000);
  const bool isC = wid < 4, isL = (wid >> 1) == 2;
  const int l31 = lane & 31, hi = lane >> 5, G = lane >> 4, q4 = (lane & 15) >> 2, p4 = lane & 3, l15 = lane & 15;
  STAMP(6);
  u64 eraw = 0, epoch = 0;
  auto load_epoch = [&]() { unsigned z = 0; OPAQUE(z); eraw = ld_flag(ws + WS_EPOCH + z); };
  auto get_epoch = [&]() {
    const u64 e = mix64(eraw);
    const unsigned e0 = __builtin_amdgcn_readfirstlane((unsigned)e), e1 = __builtin_amdgcn_readfirstlane((unsigned)(e >> 32));
    epoch = ((u64)e1 << 32) | e0;
  };
  auto wset = [&](int w, int v) { FENCE(); *(volatile LAS int*)(sm + L_W + w) = v; FENCE(); };
  auto wget = [&](int w) { return rfl(*(volatile LAS int*)(sm + L_W + w)); };
  auto wget4 = [&](int w) { const i32x4 v = *(volatile LAS i32x4*)(sm + L_W + w); return rfl(mini(mini(v[0], v[1]), mini(v[2], v[3]))); };
  auto wwait = [&](int w, int need) { for (int sp = 0; wget(w) < need && sp < SPIN_CAP; ++sp) __builtin_amdgcn_s_sleep(1); FENCE(); };
  auto wraw = [&](int w) { return *(volatile LAS unsigned*)(sm + L_W + w); };
  auto wset16 = [&](int w, int v) { FENCE(); *(volatile LAS short*)(sm + L_W + w) = (short)v; FENCE(); };
  auto wwait2 = [&](int w, unsigned first, int need) { lds_wait_halves(first, sm0 + L_W + w, need); };
  auto arrive_n = [&](int k, unsigned flag_base, unsigned n) {
    if (lane == 0) {
      const unsigned old = __hip_atomic_fetch_add((LAS unsigned*)(sm + L_W + W_CNT + 4 * k), 1u, __ATOMIC_RELAXED, __HIP_MEMORY_SCOPE_WORKGROUP);
      if (old == n - 1) {
        st_flag(ws + flag_base + (g * 16 + c1) * (k == 2 ? 8 : 128), epoch);
      }
    }
  };
  if (wid == 7) {
    u64 pc;
    asm volatile("s_getpc_b64 %0" : "=s"(pc));
    unsigned sv;
    const int ln = lane < CODE_LINES ? lane : 0;
    asm volatile("s_mov_b32 %0, m0\n\ts_mov_b32 m0, %3\n\ts_nop 2\n\tglobal_load_lds_dword %1, %2\n\ts_mov_b32 m0, %0"
                 : "=&s"(sv) : "v"((((b >> 3) & 7) * CODE_LINES + ln) * 128), "s"(pc), "s"(sm0 + L_JUNK) : "memory");
  }
  if (wid == 6) *(LAS unsigned*)(sm + L_W + 4 * lane) = 0;
  if (wid == 7) *(LAS u32x2*)(sm + L_ONES + 8 * lane) = u32x2{0x3f803f80u, 0x3f803f80u};
  if (wid == 6 && lane < 12) *(LAS u32x4*)(sm + L_KT + 16 * lane) = u32x4{lane < 8 - 4 * g ? 0x3f803f80u : 0x00003f80u, 0, 0, 0};

  auto arrive_w = [&](int k, unsigned flag_base) {
    if (lane == 0) {
      const unsigned old = __hip_atomic_fetch_add((LAS unsigned*)(sm + L_W + W_CNT + 4 * k), 1u, __ATOMIC_RELAXED, __HIP_MEMORY_SCOPE_WORKGROUP);
      if (old == 3) st_flag(ws + flag_base + wgid * 128, epoch);
    }
  };
  bf16x8 rf[10];
  unsigned rvo = 0, rstep = 0, rstep5 = 0;
  int rn = 0;
  auto wide_read = [&](unsigned st, unsigned dst, unsigned nk5) {
    LAS char* p = sm + st + q4 * 1280 + G * 320;
#pragma unroll
    for (int j = 0; j < 10; ++j) {
      const int tt = j % 5;
      LAS char* pj = p + (j / 5) * 160 + (tt < 4 ? 64 * (tt >> 1) + 16 * p4 + 8 * (tt & 1) : 128 + 8 * p4);
      rf[j] = ld_tr2(pj, pj + 4 * 1280);
    }
    rvo = dst + G * 2 * nk5 * 1024 + l15 * 16; rstep = 1024; rstep5 = nk5 * 1024; rn = 10;
  };
  auto retile_store = [&](int j) { ST128(__builtin_bit_cast(u32x4, rf[j]), rvo, (j % 5) * rstep + (j / 5) * rstep5); };
  const int su = wgid * 3 + wid - 5;
  const unsigned rst = L_RP + (wid == 4 ? 0 : 10240 + (wid - 5) * 6144);
  auto late_load = [&]() {
    if (wid == 4) {
      if (wgid < 192) {
        glds5(lane * 16, wd + (size_t)wgid * 10240, sm0 + rst);
        glds5(lane * 16, wd + (size_t)wgid * 10240 + 5120, sm0 + rst + 5120);
      }
    } else if (su < 640) {
      const size_t so = (size_t)(su >> 3) * 8 * 3072 + (su & 7) * 384;
#pragma unroll
      for (int i = 0; i < 3; ++i) {
        const int u = 64 * i + lane;
        const unsigned vr = (u / 24) * 3072 + (u % 24) * 16;
        glds(vr, wg + so, sm0 + rst + i * 1024);
        glds(vr, wu + so, sm0 + rst + 3072 + i * 1024);
      }
    }
  };
  auto late_read = [&]() {
    WAITV(0);
    if (wid == 4) {
      const int kap = wgid >> 2;
      if (wgid < 192) wide_read(rst, WS_W5 + (16 * (kap % 3) + kap / 3) * 5120 + (wgid & 3) * 256, 240);
    } else if (su < 640) {
      const int o = su >> 3;
#pragma unroll
      for (int j = 0; j < 6; ++j) {
        LAS char* p = sm + rst + (G & 1) * 3072 + q4 * 384 + (2 * j + hi) * 32 + 16 * (p4 & 1) + 8 * (p4 >> 1);
        rf[j] = ld_tr2(p, p + 4 * 384);
      }
      rvo = WS_W4 + (unsigned)((su & 7) * 480 + (o >> 1) * 2) * 1024 + (o & 1) * 512 + hi * 1024 + l31 * 16;
      rstep = 80 * 1024; rstep5 = 400 * 1024; rn = 6;
    }
    FENCE();
    if (lane == 0) __hip_atomic_fetch_add((LAS unsigned*)(sm + L_W + W_S1DONE), 1u, __ATOMIC_RELAXED, __HIP_MEMORY_SCOPE_WORKGROUP);
    FENCE();
  };
  if (isL && c1 < 15) {
    unsigned vx[8], vw[8];
#pragma unroll
    for (int i = 0; i < 8; ++i) {
      vx[i] = (lane >> 3) * 1280 + 4096 + i * (8 * 1280 - 1024) + ((((lane & 7) ^ (lane >> 4)) << 4) ^ ((i & 1) * 64));
      vw[i] = (lane >> 4) * 3840 + 4096 + i * (4 * 3840 - 1024) + (((lane & 15) ^ ((lane >> 4) << 2)) << 4);
    }
    int s = 2 + (wid & 1);
    const char* px = x + g * (128 * 1280) + s * 128;
    const char* pw = wqkv + c1 * 256 + s * (64 * 3840);
    BAR();
#pragma unroll 1
    for (; s < 10; s += 2, px += 256, pw += 128 * 3840) {
      for (int sp = 0; wget4(W_CONS1) < s - 3 && sp < SPIN_CAP; ++sp) {}
      FENCE();
      const unsigned d = sm0 + L_P1 + (s & 3) * 32768;
      glds8(vx, px, d);
      glds8(vw, pw, d + 16384);
      WAITV(16);
      if (s >= 4) wset(W_INIT + 4 * (s - 2), 0x01010101);
      STAMP(16 + s - 2);
      glds8(vx, px + 64 * 1280, d + 8192);
      glds8(vw, pw + 32 * 3840, d + 24576);
    }
    WAITV(0);
    wset(W_INIT + 4 * (s - 2), 0x01010101);
    STAMP(24);
    load_epoch();
    late_load();
    get_epoch();
    late_read();
  } else if (isC && c1 < 15) {
    const int wn = wid & 1, wt = wid >> 1;
    f32x16 acc[2][2] = {};
    const unsigned ax = L_P1 + (64 * wt + l31) * 128 + ((hi ^ ((l31 >> 1) & 7)) << 4);
    const unsigned aw = L_P1 + 16384 + (8 * hi + q4) * 256 + (((8 * wn + 2 * (G & 1) + (p4 & 1)) ^ (q4 << 2)) << 4) + 8 * (p4 >> 1);
    bf16x8 xf[4][2], wf[4][2];
    auto rd = [&](int ks, unsigned so) {
#pragma unroll
      for (int t = 0; t < 2; ++t) {
        wf[ks][t] = ld_tr2(sm + (((aw ^ (t * 64)) + so) + ks * 4096), sm + (((aw ^ (t * 64)) + so) + ks * 4096 + 1024));
        xf[ks][t] = ld_b128(sm + (((ax ^ (ks * 32)) + so) + t * 4096));
      }
    };
    auto wset8 = [&](int w) { FENCE(); *(volatile LAS char*)(sm + L_W + w) = 1; FENCE(); };
    BAR();
    WAITV(8);
    wset8(W_INIT + wid);
    lds_wait_eq(0u, sm0 + L_W + W_INIT, 0x01010101u);
    STAMP(16);
    rd(0, 0); rd(1, 0);
    SCHED();
#pragma unroll 1
    for (int s = 0; s < 10; ++s) {
      const unsigned so = (s & 3) * 32768, sn = ((s + 1) & 3) * 32768;
      unsigned lv = 0;
#pragma unroll
      for (int ks = 0; ks < 4; ++ks) {
        const int wn1 = W_INIT + 4 * (s < 9 ? s + 1 : 9);
        if (ks == 0) lv = wraw(wn1);
        if (ks == 2) {
          if (s == 0) { WAITV(0); wset8(wn1 + wid); }
          lds_wait_eq(lv, sm0 + L_W + wn1, 0x01010101u);
          STAMP2(17 + s);
          SCHED();
        }
        if (ks < 2) rd(ks + 2, so); else rd(ks - 2, sn);
        if (ks == 1) wset(W_CONS1 + 4 * wid, s + 1);
#pragma unroll
        for (int tj = 0; tj < 2; ++tj)
#pragma unroll
          for (int ti = 0; ti < 2; ++ti) acc[tj][ti] = MFMA32(wf[ks][tj], xf[ks][ti], acc[tj][ti]);
        SGB(0x8, 1); SGB(0x100, 2); SGB(0x8, 1); SGB(0x100, 2); SGB(0x8, 1); SGB(0x100, 2); SGB(0x8, 1);
        SCHED();
      }
    }
    STAMP(1);
    load_epoch();
    wwait(W_STATS, 1);
    const float sc = (c1 < 5) ? QSCALE : 1.f;
#pragma unroll
    for (int ti = 0; ti < 2; ++ti) {
      const int tok = 64 * wt + 32 * ti + l31;
      const f32x2 st = *(LAS f32x2*)(sm + L_STAT + tok * 8);
      const float r = st[1] * sc, nm = -st[0];
#pragma unroll
      for (int tj = 0; tj < 2; ++tj)
#pragma unroll
        for (int hf = 0; hf < 2; ++hf) {
          const int col = 64 * wn + 32 * tj + 16 * hf + 8 * hi;
          const f32x4 ca = *(LAS f32x4*)(sm + L_CS + col * 4), cb = *(LAS f32x4*)(sm + L_CS + col * 4 + 16);
          float v[8];
#pragma unroll
          for (int j = 0; j < 4; ++j) {
            v[j] = (acc[tj][ti][8 * hf + j] + nm * ca[j]) * r;
            v[4 + j] = (acc[tj][ti][8 * hf + 4 + j] + nm * cb[j]) * r;
          }
          u32x4 o = {pk2(v[0], v[1]), pk2(v[2], v[3]), pk2(v[4], v[5]), pk2(v[6], v[7])};
          ST128(o, hi * 2048 + tok * 16, WS_QKV + (unsigned)(g * 240 + 16 * c1 + 8 * wn + 4 * tj + 2 * hf) * 2048);
        }
    }
    get_epoch();
    WAITV(0);
    arrive_n(1, WS_F1, 4);
  } else {
    const int sw = wid & 1;
    const bool srv = wid >= 6;
    load_epoch();
    u32x4 v[10];
    if (srv) {
      const char* src = sw ? wqkv + (size_t)(40 * g) * 3840 + (c1 < 15 ? c1 : 0) * 256 : x + (size_t)(128 * g + 8 * c1) * 1280;
      const unsigned vs = sw ? G * 3840 + l15 * 16 : (lane >> 3) * 1280 + (lane & 7) * 16, st = sw ? 4 * 3840 : 128;
#pragma unroll
      for (int i = 0; i < 10; ++i) v[i] = *(const u32x4*)(src + i * st + vs);
    }
    SCHED();
    BAR();
    if (srv) {
      STAMP(15);
      if (sw == 0) {
        float s = 0.f, q = 0.f;
#pragma unroll
        for (int i = 0; i < 10; ++i)
#pragma unroll
          for (int j = 0; j < 4; ++j) { s = dot2(v[i][j], 0x3f803f80u, s); q = dot2(v[i][j], v[i][j], q); }
        s = sum8(s); q = sum8(q);
        const float mu = s * (1.f / 640.f);
        const float rstd = __builtin_amdgcn_rsqf(fmaxf(q * (1.f / 640.f) - mu * mu, 0.f) + 1e-5f);
        if ((lane & 7) == 0) ST64((u32x2{__float_as_uint(mu), __float_as_uint(rstd)}), lane, WS_ST1 + (unsigned)(128 * g + 8 * c1) * 8);
      } else {
        float a[8] = {};
#pragma unroll
        for (int i = 0; i < 10; ++i)
#pragma unroll
          for (int j = 0; j < 4; ++j) { a[2 * j] += bflo(v[i][j]); a[2 * j + 1] += bfhi(v[i][j]); }
#pragma unroll
        for (int j = 0; j < 8; ++j) a[j] = sum32(sum16(a[j]));
        if (lane < 32) {
          const f32x4 o = (lane & 16) ? f32x4{a[4], a[5], a[6], a[7]} : f32x4{a[0], a[1], a[2], a[3]};
          ST128(__builtin_bit_cast(u32x4, o), l15 * 32 + (lane & 16), WS_CS1 + (unsigned)(c1 * 16 + g) * 512);
        }
      }
      get_epoch();
      WAITV(0);
      arrive_n(0, WS_F0, 2);
      STAMP(16);
    } else {
      get_epoch();
      if (wid < 4) {
        wwait(W_CNT, 2);
        const int cc = tid % 24, rg = tid / 24;
        float a[8] = {};
        if (rg < 10) {
          const char* src = (g < 8 ? wg : wu) + (g & 7) * 384;
          const unsigned vs = rg * 3072 + cc * 16;
#pragma unroll 1
          for (int hlf = 0; hlf < 2; ++hlf) {
            u32x4 w[32];
#pragma unroll
            for (int i = 0; i < 32; ++i) w[i] = *(const u32x4*)(src + (hlf * 32 + i) * 30720 + vs);
            SCHED();
#pragma unroll
            for (int i = 0; i < 32; ++i)
#pragma unroll
              for (int j = 0; j < 4; ++j) { a[2 * j] += bflo(w[i][j]); a[2 * j + 1] += bfhi(w[i][j]); }
          }
          *(LAS f32x4*)(sm + L_HSUM + (rg * 24 + cc) * 32) = f32x4{a[0], a[1], a[2], a[3]};
          *(LAS f32x4*)(sm + L_HSUM + (rg * 24 + cc) * 32 + 16) = f32x4{a[4], a[5], a[6], a[7]};
        }
        FENCE();
        unsigned old = 0;
        if (lane == 0) old = __hip_atomic_fetch_add((LAS unsigned*)(sm + L_W + W_CNT + 28), 1u, __ATOMIC_RELAXED, __HIP_MEMORY_SCOPE_WORKGROUP);
        if (rfl(old) == 3) {
          FENCE();
          if (lane < 48) {
            f32x4 s = {};
#pragma unroll 5
            for (int r = 0; r < 10; ++r) s += *(LAS f32x4*)(sm + L_HSUM + r * 768 + lane * 16);
            ST128(__builtin_bit_cast(u32x4, s), (lane >> 2) * 128 + (lane & 3) * 16, WS_CSUM + (unsigned)((g & 7) * 1536 + (g >> 3) * 64));
          }
        }
      }
    }
    if (c1 == 15 && wid >= 4) {
#pragma unroll 1
      for (int u = wid - 4; u < 5; u += 4) {
        const int o = 5 * g + u;
        const unsigned stg = L_P1 + wid * 10240;
        glds5(lane * 16, wo + (size_t)o * 10240, sm0 + stg);
        glds5(lane * 16, wo + (size_t)o * 10240 + 5120, sm0 + stg + 5120);
        WAITV(0);
        wide_read(stg, WS_W3 + (o >> 2) * 5120 + (o & 3) * 256, 100);
#pragma unroll
        for (int j = 0; j < 10; ++j) retile_store(j);
      }
      rn = 0;
      WAITV(0);
      arrive_n(1, WS_F1, 4);
    }
    if (wid == 7 && c1 < 15) {
      wait_flags(ws + WS_F0 + (lane < 16 ? g * 16 + lane : (l15 * 16 + c1)) * 128, epoch);
      const u32x4 gs = LD128(lane * 16, WS_ST1 + (unsigned)g * 1024);
      f32x4 t = {};
#pragma unroll
      for (int i = 0; i < 8; ++i) t += __builtin_bit_cast(f32x4, LD128(hi * 4096 + l31 * 16, WS_CS1 + (unsigned)(c1 * 16 + i) * 512));
#pragma unroll
      for (int j = 0; j < 4; ++j) t[j] = sum32(t[j]);
      *(LAS u32x4*)(sm + L_STAT + lane * 16) = gs;
      if (lane < 32) *(LAS f32x4*)(sm + L_CS + lane * 16) = t;
      wset(W_STATS, 1);
      STAMP(19);
    }
    if (wid >= 4) { late_load(); late_read(); }
    STAMP(17);
    if (wid == 6) {
      if (c1 < 15) { for (int sp = 0; wget4(W_CONS1) < 10 && sp < SPIN_CAP; ++sp) __builtin_amdgcn_s_sleep(1); }
      wwait(W_S1DONE, 4);
      const int i = lane < 18 ? lane : 0;
      const int which = i / 6, po = which ? (i % 6) >> 1 : 0, e = i & 1;
      const int tile = (640 * which + 40 * c + 39 * e) >> 7;
      const int pn = g - po < 0 ? 0 : g - po;
      wait_flags(ws + WS_F1 + (pn * 16 + tile) * 128, epoch);
      wset(W_GO, 1);
    }
  }
  const char* wsrc3 = ws + WS_W3 + (c & 7) * 102400 + (wid & 1) * 10240;
  const char* wsrc4 = ws + WS_W4 + c * 245760 + (wid & 1) * 10240 - 5 * 20480;
  const char* wsrc5 = ws + WS_W5 + (c & 7) * 245760 + (wid & 1) * 10240 - 17 * 20480;
  auto wissue_half = [&](int st, int h) {
    const char* sb = (st < 5 ? wsrc3 : st < 17 ? wsrc4 : wsrc5) + st * 20480 + h * 5120;
    glds5_sc1(lane * 16, sb, sm0 + L_RING + ((st + 3) % 7) * RSLOT + (wid & 1) * 10240 + h * 5120);
  };
  STAMP(2);

  bool wready;
  {
    const int hs = wid >> 2, qb = wid & 3;
    if (!isC) {
      const int arr = 1 + ((wid >> 1) & 1), bk0 = (wid & 1) * 2;
      const unsigned dstr = arr == 1 ? 6144 : VSTR, dbase = sm0 + (arr == 1 ? L_K : L_V);
      const char* sA = ws + WS_QKV + (size_t)((g - bk0 < 0 ? g : g - bk0) * 240 + 80 * arr + 5 * c) * 2048;
      const char* sB = ws + WS_QKV + (size_t)(wid == 5 ? g * 240 + 5 * c : (g < 1 ? g : g - 1) * 240 + 80 * arr + 5 * c) * 2048;
      const unsigned dA = dbase + (2 - bk0) * 2048, dB = wid == 5 ? sm0 + L_OST : dbase + 2048, dstrB = wid == 5 ? 2048 : dstr;
      wwait(W_GO, 1);
      STAMP(3);
#pragma unroll
      for (int j = 0; j < 5; ++j) {
        glds2_sc1(lane * 16, sA + j * 2048, dA + j * dstr);
        if (wid != 7) glds2_sc1(lane * 16, sB + j * 2048, dB + j * dstrB);
      }
      if (wid == 7) {
        if (__all(ld_flag(ws + WS_F1 + (l15 * 16 + 15) * 128) == epoch)) wset(W_WREADY, 1);
      }
    }
    const unsigned ak0 = L_K + (3 * hs + hi) * 6144 + l31 * 16, ak1 = L_K + 2 * 6144 + l31 * 16;
    const int kb_lo = (g >= 2) ? qb : (g == 1 ? 4 : 8), kb_hi = qb + 8;
    const float NINF = -__builtin_inff();
    f32x16 cD, cF;
#pragma unroll
    for (int r = 0; r < 16; ++r) {
      const bool low = (r & 3) + 8 * (r >> 2) + 4 * hi <= l31;
      cD[r] = low ? 0.f : NINF;
      cF[r] = low ? NINF : 0.f;
    }
    const bool isOne = (G & 1) && p4;
    const unsigned av0 = isOne ? L_ONES
                               : L_V + (4 * hi + q4) * 16 + ((G & 1) ? 2 * VSTR + 8 * hs : (3 * hs + (p4 & 1)) * VSTR + 8 * (p4 >> 1));
    const unsigned avs = isOne ? 0 : 512;
    unsigned a0 = ak0 + qb * 512, a1 = hi ? L_KT + qb * 16 : ak1 + qb * 512, av = av0 + qb * avs;
    const unsigned a1s = hi ? 16 : 512;
    asm volatile("" : "+v"(cD), "+v"(cF), "+v"(a0), "+v"(a1), "+v"(av));
    WAITV(0);
    BAR();
    STAMP(4);
    wready = isL && wget(W_WREADY);
    const int row = 32 * qb + l31;
    u32x4 q0 = *(LAS u32x4*)(sm + L_OST + (3 * hs + hi) * 2048 + row * 16), q1 = *(LAS u32x4*)(sm + L_OST + 2 * 2048 + row * 16);
    if (hi || hs) { q1[0] = 0; q1[1] = 0; }
    if (hi || !hs) { q1[2] = 0; q1[3] = 0; }
    const bf16x8 qf0 = __builtin_bit_cast(bf16x8, q0), qf1 = __builtin_bit_cast(bf16x8, q1);
    auto vmax = [&](const f32x16& s, float m) {
#pragma unroll
      for (int r = 0; r < 16; r += 2) m = fmaxf(fmaxf(m, s[r]), s[r + 1]);
      return m;
    };
    f32x16 o;
    float rl, m;
    auto finish = [&]() {
      auto sw = __builtin_amdgcn_permlane32_swap(__float_as_uint(o[8]), __float_as_uint(o[8]), false, false);
      const float l = __uint_as_float(sw[1]);
      rl = __builtin_amdgcn_rcpf(l);
      float am = 0.f;
#pragma unroll
      for (int r = 0; r < 12; r += 2) am = fmaxf(fmaxf(am, fabsf(o[r])), fabsf(o[r + 1]));
      return (bool)__all(l <= 0x1p30f && l >= 0x1p-30f && am < __builtin_inff());
    };
    {
      f32x16 s[2];
      bf16x8 k0 = ld_b128(sm + a0 + 8 * 512), k1 = ld_b128(sm + a1 + 8 * a1s);
      s[1] = mfma32_c(k0, qf0, cD);
      s[0] = mfma32_c(k1, qf1, s[1]);
      asm volatile("s_nop 11" : "+v"(s[0]));
      m = max32(vmax(s[0], NINF));
      u32x4 q2 = q1;
      if (hi) q2[0] = pk2(-m, -3.0e38f);
      const bf16x8 qf2 = __builtin_bit_cast(bf16x8, q2);
      s[0] = MFMA32(k1, qf2, s[1]);
      k0 = ld_b128(sm + a0); k1 = ld_b128(sm + a1);
      o = f32x16{};
#pragma unroll
      for (int j = 0; j < 9; ++j) {
        f32x16& sc = s[j & 1];
        f32x16& sn = s[(j + 1) & 1];
        unsigned avj = av + (j == 0 ? 8 : j - 1) * avs;
        bf16x8 vf0 = ld_tr2(sm + avj, sm + avj + 128), vf1 = ld_tr2(sm + avj + 256, sm + avj + 384);
        unsigned pw[8];
        if (j < 8) sn = MFMA32(k0, qf0, j == 0 ? cF : f32x16{});
        SCHED();
#pragma unroll
        for (int r = 0; r < 2; ++r) pw[r] = pk2(__builtin_amdgcn_exp2f(sc[2 * r]), __builtin_amdgcn_exp2f(sc[2 * r + 1]));
        SCHED();
        if (j < 8) sn = MFMA32(k1, qf2, sn);
        if (j < 7) { a1 += a1s; k0 = ld_b128(sm + a0 + (j + 1) * 512); k1 = ld_b128(sm + a1); }
        SCHED();
#pragma unroll
        for (int r = 2; r < 5; ++r) pw[r] = pk2(__builtin_amdgcn_exp2f(sc[2 * r]), __builtin_amdgcn_exp2f(sc[2 * r + 1]));
        SCHED();
        o = MFMA32(vf0, __builtin_bit_cast(bf16x8, (u32x4{pw[0], pw[1], pw[2], pw[3]})), o);
        SCHED();
#pragma unroll
        for (int r = 5; r < 8; ++r) pw[r] = pk2(__builtin_amdgcn_exp2f(sc[2 * r]), __builtin_amdgcn_exp2f(sc[2 * r + 1]));
        SCHED();
        o = MFMA32(vf1, __builtin_bit_cast(bf16x8, (u32x4{pw[4], pw[5], pw[6], pw[7]})), o);
        SCHED();
        if (j < 5 && 2 * j < rn) { retile_store(2 * j); retile_store(2 * j + 1); }
        if (j < 8 && wready) wissue_half(j >> 1, j & 1);
      }
    }
    if (!finish()) {
      int lq = l31 - 4 * hi;
      OPAQUE(lq);
#pragma unroll 1
      for (int k2 = kb_lo; k2 < kb_hi; ++k2) {
        f32x16 cz;
#pragma unroll
        for (int r = 0; r < 16; ++r) cz[r] = (k2 == qb && (r & 3) + 8 * (r >> 2) <= lq) ? NINF : 0.f;
        f32x16 s = MFMA32(ld_b128(sm + ak0 + k2 * 512), qf0, cz);
        s = MFMA32(ld_b128(sm + ak1 + k2 * 512), qf1, s);
        m = vmax(s, m);
      }
      m = max32(m);
      o = f32x16{};
#pragma unroll 1
      for (int kb = kb_lo; kb <= kb_hi; ++kb) {
        f32x16 cin;
#pragma unroll
        for (int r = 0; r < 16; ++r) cin[r] = ((r & 3) + 8 * (r >> 2) <= lq ? kb == qb : kb == kb_hi) ? NINF : -m;
        f32x16 s = MFMA32(ld_b128(sm + ak0 + kb * 512), qf0, cin);
        s = MFMA32(ld_b128(sm + ak1 + kb * 512), qf1, s);
        const unsigned av = av0 + kb * avs;
        bf16x8 vf0 = ld_tr2(sm + av, sm + av + 128), vf1 = ld_tr2(sm + av + 256, sm + av + 384);
        unsigned pw[8];
#pragma unroll
        for (int r = 0; r < 8; ++r) pw[r] = pk2(__builtin_amdgcn_exp2f(s[2 * r]), __builtin_amdgcn_exp2f(s[2 * r + 1]));
        o = MFMA32(vf0, __builtin_bit_cast(bf16x8, (u32x4{pw[0], pw[1], pw[2], pw[3]})), o);
        o = MFMA32(vf1, __builtin_bit_cast(bf16x8, (u32x4{pw[4], pw[5], pw[6], pw[7]})), o);
      }
      (void)finish();
    }
    *(LAS u32x4*)(sm + L_OST + (3 * hs + hi) * 2048 + row * 16) =
        u32x4{pk2(o[0] * rl, o[1] * rl), pk2(o[2] * rl, o[3] * rl), pk2(o[4] * rl, o[5] * rl), pk2(o[6] * rl, o[7] * rl)};
    if (!hi) *(LAS u32x2*)(sm + L_OST + 2 * 2048 + row * 16 + 8 * hs) = u32x2{pk2(o[8] * rl, o[9] * rl), pk2(o[10] * rl, o[11] * rl)};
    STAMP(5);
    BAR();
    if (!isC) { WAITV(0); arrive_n(2, WS_F1P, 4); }
  }

  if (isL) {
    int issued = 4;
    if (!wready) {
#pragma unroll 1
      for (int i = 0; i < 1; ++i) wait_flags(ws + WS_F1 + (l15 * 16 + 15) * 128, epoch);
      issued = 0;
    }
    const int wl = W_LANDW + 2 * (wid & 1);
    for (; issued < 5; ++issued) { const int st = rfl(issued); wissue_half(st, 0); wissue_half(st, 1); }
    WAITV(0);
    wset16(wl, 5);
    for (int sp = 0; wget4(W_CONSW) < 1 && sp < SPIN_CAP; ++sp) __builtin_amdgcn_s_sleep(2);
    {
      u64 f[4];
#pragma unroll
      for (int i = 0; i < 4; ++i) f[i] = ld_flag(ws + WS_F1P + i * 512 + lane * 8);
#pragma unroll
      for (int i = 0; i < 4; ++i) wait_flags_from(ws + WS_F1P + i * 512 + lane * 8, f[i], epoch);
    }
    STAMP(30);
    if (wid == 4 && lane < 48) glds_sc1(lane * 16, ws + WS_CSUM + c * 768, sm0 + L_CSUM4);
    const char* p = wsrc4 + 5 * 20480;
    unsigned dst = sm0 + L_RING + RSLOT + (wid & 1) * 10240;
#pragma unroll 1
    for (int st = 5; st < 29; ++st) {
      for (int sp = 0; wget4(W_CONSW) < (st < 7 ? 1 : st - 5) && sp < SPIN_CAP; ++sp) __builtin_amdgcn_s_sleep(1);
      FENCE();
      glds10_sc1(lane * 16, p, dst);
      p = st == 16 ? wsrc5 + 17 * 20480 : p + 20480;
      dst = dst + RSLOT >= sm0 + L_RING + 7 * RSLOT ? dst - 6 * RSLOT : dst + RSLOT;
      WAITV(20);
      if (st >= 7) wset16(wl, st - 1);
      STAMP(31 + st - 1);
    }
    WAITV(10);
    wset16(wl, 28);
    WAITV(0);
    wset16(wl, 29);
    STAMP(60);
    STAMP_DUMP();
    return;
  }
  if (!isC) {
    if (wid == 6) {
      for (int sp = 0; wget(W_RDY) < 2 && sp < SPIN_CAP; ++sp) __builtin_amdgcn_s_sleep(4);
      FENCE();
      {
        f32x4 t = {};
#pragma unroll
        for (int i = 0; i < 8; ++i) t += __builtin_bit_cast(f32x4, LD128(lane * 16, WS_STAT + (unsigned)(g * 8 + i) * 1024));
        t *= 1.f / 640.f;
        *(LAS f32x4*)(sm + L_ST4 + lane * 16) = f32x4{-t[0], __builtin_amdgcn_rcpf(fmaxf(t[1] - t[0] * t[0], 0.f) + 1e-5f),
                                                      -t[2], __builtin_amdgcn_rcpf(fmaxf(t[3] - t[2] * t[2], 0.f) + 1e-5f)};
        wset(W_STATS, 2);
      }
#pragma unroll 1
      for (int k = 0; k < 3; ++k) {
        for (int sp = 0; wget4(W_DONE) <= k && sp < SPIN_CAP; ++sp) __builtin_amdgcn_s_sleep(2);
        wait_flags(ws + WS_F4 + k * FL + (g * 16 + l15) * 128, epoch);
        wset(W_RDY, k + 3);
        STAMP(22 + k);
      }
    } else {
#pragma unroll 1
      for (int k = 0; k < 3; ++k) {
        for (int sp = 0; wget4(W_DONE) <= k && sp < SPIN_CAP; ++sp) __builtin_amdgcn_s_sleep(1);
        FENCE();
        if (lane == 0) st_flag(ws + WS_F4 + k * FL + wgid * 128, epoch);
        STAMP(22 + k);
      }
      if (b == 0) {
#pragma unroll 1
        for (int i = 0; i < 4; ++i) wait_flags(ws + WS_F2 + (i * 64 + lane) * 128, epoch);
        if (lane == 0) st_flag(ws + WS_EPOCH, epoch);
      }
    }
    STAMP_DUMP();
    return;
  }

#pragma unroll 1
  for (int pi = wid; pi < 10; pi += 4) {
    u32x4 v = *(LAS u32x4*)(sm + L_OST + pi * 1024 + lane * 16);
    ST128(v, lane * 16, WS_O + (unsigned)(g * 80 + 5 * c) * 2048 + pi * 1024);
  }
  WAITV(0);
  const int wdn = W_DONE + 4 * wid;
  arrive_w(3, WS_F2);
  STAMP(6);
  const int wc = W_CONSW + 4 * wid;
  const unsigned lane16 = lane * 16;

  const int r3 = 64 * (c >> 3) + 16 * wid + l15;
  {
    unsigned hpk[10];
    const char* xp = x + (size_t)(128 * g + r3) * 1280 + (c & 7) * 160;
    const u32x4 xr0 = *(const u32x4*)(xp + G * 16), xr1 = *(const u32x4*)(xp + 64 + G * 16);
    const u32x2 xr2 = *(const u32x2*)(xp + 128 + G * 8);
    wait_flags(ws + WS_F2 + (g * 16 + l15) * 128, epoch);
    STAMP(7);
    u32x4 xf[20];
#pragma unroll
    for (int s = 0; s < 20; ++s) xf[s] = LD128(G * 2048 + r3 * 16, WS_O + (unsigned)(g * 80 + 4 * s) * 2048);
    wset(wc, 1);
    f32x4 acc[5] = {};
    {
      constexpr int NB = 15;
      bf16x8 wf[NB];
      unsigned so = 0;
      auto rd = [&](int f) {
        if (f % 20 == 0) { so = lane16; OPAQUE(so); so += slot_of(f / 20); OPAQUE(so); }
        wf[f % NB] = ld_b128(sm + so + (f % 20) * 1024);
        if (f % 20 == 19 && f < 20 * P3REL) wset(wc, f / 20 + 2);
      };
      wwait2(W_LANDW, 0u, 5);
#pragma unroll
      for (int f = 0; f < NB; ++f) rd(f);
      SCHED();
#pragma unroll
      for (int f = 0; f < 100; ++f) {
        acc[f % 5] = MFMA16(wf[f % NB], __builtin_bit_cast(bf16x8, xf[f / 5]), acc[f % 5]);
        SCHED();
        if (f + NB < 100) rd(f + NB);
        SCHED();
      }
    }
    STAMP(8);
    float s1 = 0.f, s2 = 0.f;
#pragma unroll
    for (int d = 0; d < 10; ++d) {
      const unsigned xv = d < 4 ? xr0[d & 3] : d < 8 ? xr1[d & 3] : xr2[d & 1];
      const unsigned a = pk2(acc[d >> 1][2 * (d & 1)], acc[d >> 1][2 * (d & 1) + 1]);
      const unsigned h = pk2(bflo(a) + bflo(xv), bfhi(a) + bfhi(xv));
      hpk[d] = h;
      s1 += bflo(h) + bfhi(h);
      s2 += bflo(h) * bflo(h) + bfhi(h) * bfhi(h);
    }
    const unsigned hb = WS_H + (unsigned)(g * 80 + 10 * (c & 7)) * 2048;
    ST128((u32x4{hpk[0], hpk[1], hpk[2], hpk[3]}), G * 2048 + r3 * 16, hb);
    ST128((u32x4{hpk[4], hpk[5], hpk[6], hpk[7]}), G * 2048 + r3 * 16, hb + 4 * 2048);
    {
      auto a0 = __builtin_amdgcn_permlane16_swap(hpk[8], hpk[8], false, false);
      auto a1 = __builtin_amdgcn_permlane16_swap(hpk[9], hpk[9], false, false);
      if (!(G & 1)) ST128((u32x4{a0[0], a1[0], a0[1], a1[1]}), (G >> 1) * 2048 + r3 * 16, hb + 8 * 2048);
    }
    s1 = sum32(sum16(s1)); s2 = sum32(sum16(s2));
    if (G == 0) ST64((u32x2{__float_as_uint(s1), __float_as_uint(s2)}), r3 * 8, WS_STAT + (unsigned)(g * 8 + (c & 7)) * 1024);
    *(LAS u32x4*)(sm + L_OST + wid * 2560 + lane * 16) = u32x4{hpk[0], hpk[1], hpk[2], hpk[3]};
    *(LAS u32x4*)(sm + L_OST + wid * 2560 + 1024 + lane * 16) = u32x4{hpk[4], hpk[5], hpk[6], hpk[7]};
    *(LAS u32x2*)(sm + L_OST + wid * 2560 + 2048 + lane * 8) = u32x2{hpk[8], hpk[9]};
    WAITV(0);
    arrive_w(4, WS_F3);
    STAMP(9);
  }

  const unsigned hb5 = WS_HH + (unsigned)(g * 192) * 2048;
  unsigned vx5, ln5;
  u32x4 xa[16];
  {
    wait_flags(ws + WS_F3 + (g * 16 + l15) * 128, epoch);
    if (wid == 0) wset(W_RDY, 2);
    STAMP(10);
    const int r4 = 32 * wid + l31;
    const unsigned vx = hi * 2048 + r4 * 16;
    const unsigned hb = WS_H + (unsigned)(g * 80) * 2048;
    u32x4 xf[40];
    constexpr int XW = 14;
#pragma unroll
    for (int i = 0; i < XW; ++i) xf[i] = LD128(vx, hb + i * 4096);
    wset(wc, 6);
    float r2 = 0.f, nmu = 0.f;
    constexpr int NB = 6;
    bf16x8 wf[NB];
    unsigned so = 0;
    unsigned lv = 0;
    auto rd = [&](int f) {
      if (f % 20 == 0) { wwait2(W_LANDW, lv, 5 + f / 20 + 1); so = lane16; OPAQUE(so); so += slot_of(5 + f / 20); OPAQUE(so); }
      wf[f % NB] = ld_b128(sm + so + (f % 20) * 1024);
      if (f % 20 == 12) lv = wraw(W_LANDW);
      if (f % 20 == 19) wset(wc, 5 + f / 20 + 2);
    };
#pragma unroll
    for (int f = 0; f < NB; ++f) rd(f);
    SCHED();
#pragma unroll
    for (int part = 0; part < 3; ++part) {
      f32x16 acc[2] = {};
#pragma unroll
      for (int i = 0; i < 80; ++i) {
        const int f = 80 * part + i;
        if (f % 20 == 0 && f > 0) { STAMP2(42 + f / 20); SCHED(); }
        acc[i & 1] = MFMA32(wf[f % NB], __builtin_bit_cast(bf16x8, xf[i >> 1]), acc[i & 1]);
        SCHED();
        if (f + NB < 240) rd(f + NB);
        if (part == 0 && (i & 1) && (i >> 1) + XW < 40) xf[(i >> 1) + XW] = LD128(vx, hb + ((i >> 1) + XW) * 4096);
        SCHED();
        if (part > 0 && i == 40) { WAITV(0); wset(wdn, part); SCHED(); }
        if (part == 2 && i == 46) {
          ln5 = lane16; OPAQUE(ln5);
          vx5 = (ln5 >> 8) * 2048 + (64 * (c >> 3) + 16 * wid) * 16 + (ln5 & 255);
          lds_wait_ge(sm0 + L_W + W_RDY, 3);
          SCHED();
        }
        if (part == 2 && i >= 47 && i < 79 && (i & 1)) { xa[(i - 47) >> 1] = LD128(vx5, hb5 + (3 * ((i - 47) >> 1)) * 8192); SCHED(); }
      }
      if (part == 0) {
        wwait(W_STATS, 2);
        const f32x2 st = *(LAS f32x2*)(sm + L_ST4 + r4 * 8);
        nmu = st[0]; r2 = st[1];
        SCHED();
      }
      if (part == 2) STAMP(11);
#pragma unroll
      for (int t = 0; t < 2; ++t) {
        const int tt = 2 * part + t;
        LAS char* cp = sm + L_CSUM4 + tt * 128 + hi * 32;
        const f32x4 g0 = *(LAS f32x4*)cp, g1 = *(LAS f32x4*)(cp + 16), u0 = *(LAS f32x4*)(cp + 64), u1 = *(LAS f32x4*)(cp + 80);
        unsigned o[4];
#pragma unroll
        for (int d = 0; d < 4; ++d) {
          const f32x4 gc = d < 2 ? g0 : g1, uc = d < 2 ? u0 : u1;
          const int e = (2 * d) & 3;
          const f32x2 gv = f32x2{acc[t][2 * d], acc[t][2 * d + 1]} + nmu * f32x2{gc[e], gc[e + 1]};
          const f32x2 uv = f32x2{acc[t][8 + 2 * d], acc[t][8 + 2 * d + 1]} + nmu * f32x2{uc[e], uc[e + 1]};
          const f32x2 hv = (gv * r2) * uv;
          o[d] = pk2(hv[0], hv[1]);
        }
        ST128((u32x4{o[0], o[1], o[2], o[3]}), vx, WS_HH + (unsigned)(g * 192 + 12 * c + 2 * tt) * 2048);
        SCHED();
      }
      SCHED();
    }
    STAMP(12);
  }

  {
    f32x4 acc[5] = {};
    constexpr int NB = 15;
    bf16x8 wf[NB];
    unsigned so = 0;
    unsigned lv = 0;
    auto rd = [&](int f) {
      if (f % 20 == 0) { wwait2(W_LANDW, lv, 17 + f / 20 + 1); so = lane16; OPAQUE(so); so += slot_of(17 + f / 20); OPAQUE(so); }
      wf[f % NB] = ld_b128(sm + so + (f % 20) * 1024);
      if (f % 20 == 6) lv = wraw(W_LANDW);
      if (f % 20 == 19) wset(wc, 17 + f / 20 + 2);
    };
    u32x4 xb[16];
#pragma unroll
    for (int f = 0; f < NB; ++f) rd(f);
    SCHED();
#pragma unroll
    for (int part = 0; part < 3; ++part) {
#pragma unroll
      for (int i = 0; i < 80; ++i) {
        const int f = 80 * part + i;
        acc[i % 5] = MFMA16(wf[f % NB], __builtin_bit_cast(bf16x8, part == 1 ? xb[i / 5] : xa[i / 5]), acc[i % 5]);
        SCHED();
        if (f + NB < 240) rd(f + NB);
        SCHED();
        if (f == 47) { WAITV(0); wset(wdn, 3); STAMP(13); lds_wait_ge(sm0 + L_W + W_RDY, 4); STAMP(40); SCHED(); }
        if (f >= 48 && f < 80 && !(f & 1)) { xb[(f - 48) >> 1] = LD128(vx5, hb5 + (3 * ((f - 48) >> 1) + 1) * 8192); SCHED(); }
        if (f == 127) { lds_wait_ge(sm0 + L_W + W_RDY, 5); STAMP(41); SCHED(); }
        if (f >= 128 && f < 160 && !(f & 1)) { xa[(f - 128) >> 1] = LD128(vx5, hb5 + (3 * ((f - 128) >> 1) + 2) * 8192); SCHED(); }
      }
    }
    STAMP(14);
    unsigned opk[10];
    const u32x4 h0 = *(LAS u32x4*)(sm + L_OST + wid * 2560 + ln5), h1 = *(LAS u32x4*)(sm + L_OST + wid * 2560 + 1024 + ln5);
    const u32x2 h2 = *(LAS u32x2*)(sm + L_OST + wid * 2560 + 2048 + (ln5 >> 1));
#pragma unroll
    for (int d = 0; d < 10; ++d) {
      const unsigned hv = d < 4 ? h0[d & 3] : d < 8 ? h1[d & 3] : h2[d & 1];
      const unsigned a = pk2(acc[d >> 1][2 * (d & 1)], acc[d >> 1][2 * (d & 1) + 1]);
      opk[d] = pk2(bflo(a) + bflo(hv), bfhi(a) + bfhi(hv));
    }
    const unsigned G5 = ln5 >> 8;
    char* op = out + (size_t)(128 * g + 64 * (c >> 3) + 16 * wid) * 1280 + (c & 7) * 160 + (ln5 & 255) * 80;
    *(u32x4*)(op + G5 * 16) = u32x4{opk[0], opk[1], opk[2], opk[3]};
    *(u32x4*)(op + 64 + G5 * 16) = u32x4{opk[4], opk[5], opk[6], opk[7]};
    *(u32x2*)(op + 128 + G5 * 8) = u32x2{opk[8], opk[9]};
    STAMP(15);
    STAMP_DUMP();
  }
}

extern "C" void kernel_launch(void* const* d_in, const int* in_sizes, int n_in, void* d_out, int out_size, void* d_ws, size_t ws_size,
                              hipStream_t stream) {
  fused_block<<<256, 512, 0, stream>>>((const char*)d_in[0], (const char*)d_in[1], (const char*)d_in[2], (const char*)d_in[4],
                                       (const char*)d_in[5], (const char*)d_in[6], (char*)d_out, (char*)d_ws);
}
```

```cpp
#include <hip/hip_runtime.h>
#include <stdint.h>

typedef unsigned long long u64;
typedef __attribute__((ext_vector_type(8))) short bf16x8;
typedef __attribute__((ext_vector_type(4))) short s16x4;
typedef __attribute__((ext_vector_type(2))) __bf16 bf16x2;
typedef __attribute__((ext_vector_type(16))) float f32x16;
typedef __attribute__((ext_vector_type(4))) float f32x4;
typedef __attribute__((ext_vector_type(2))) float f32x2;
typedef __attribute__((ext_vector_type(4))) unsigned u32x4;
typedef __attribute__((ext_vector_type(2))) unsigned u32x2;
#define LAS __attribute__((address_space(3)))
#define DI __device__ __forceinline__

constexpr unsigned WS_EPOCH = 0, WS_HINT = 1024;
constexpr unsigned FL = 256 * 128;
constexpr unsigned WS_F0 = 4096, WS_F1 = WS_F0 + FL, WS_F2 = WS_F1 + FL, WS_F3 = WS_F2 + FL, WS_F4 = WS_F3 + FL;
constexpr unsigned WS_F1P = WS_F4 + 3 * FL;
constexpr unsigned WS_CSUM = WS_F1P + 2048;
constexpr unsigned WS_ST1 = WS_CSUM + 27 * 12288;
constexpr unsigned WS_CS1 = WS_ST1 + 2048 * 8;
constexpr unsigned WS_STAT = WS_CS1 + 16 * 16 * 512;
constexpr unsigned WS_QKV = 1u << 20;
constexpr unsigned WS_O = WS_QKV + 16 * 240 * 2048;
constexpr unsigned WS_H = WS_O + 16 * 80 * 2048;
constexpr unsigned WS_HH = WS_H + 16 * 80 * 2048;
constexpr unsigned WS_W3 = WS_HH + 16 * 192 * 2048;
constexpr unsigned WS_W4 = WS_W3 + 8 * 100 * 1024;
constexpr unsigned WS_W5 = WS_W4 + 16 * 240 * 1024;
constexpr unsigned WS_END = WS_W5 + 8 * 240 * 1024;
constexpr unsigned WS_DIAG = 32u << 20;
static_assert(WS_STAT + 16 * 8 * 1024 <= WS_QKV && WS_END <= WS_DIAG, "workspace map");

constexpr unsigned L_STAT = 0, L_CS = 1024;
constexpr unsigned L_W = 1536;
constexpr unsigned L_ONES = 1792;
constexpr unsigned L_KT = 2304;
constexpr unsigned L_RP = 2560;
constexpr unsigned L_OST = 2560;
constexpr unsigned L_JUNK = 31488;
constexpr unsigned L_HSTG = 81920;
constexpr unsigned L_WX = 12800;
constexpr int CODE_LINES = 48;
constexpr unsigned L_P1 = 32768;
constexpr unsigned L_K = 12800, L_V = L_K + 5 * 6144, VSTR = 6208;
constexpr unsigned L_RING = 20480, RSLOT = 20480;
constexpr unsigned slot_of(int i) { return L_RING + ((i + 3) % 7) * RSLOT; }
static_assert(L_V + 5 * VSTR <= L_RING + 3 * RSLOT, "LDS map");
enum { W_LAND1 = 0, W_LANDW = 8, W_CONS1 = 16, W_CONSW = 32, W_GO = 48, W_S1DONE = 52, W_STATS = 56, W_WREADY = 60, W_RDY = 64, W_DONE = 80, W_CNT = 96, W_INIT = 128 };

#define P4REL 0
#define APRIO 2
#define PDLY 24
#define PSLP 1
#define GDLY 16
#define FDLY 24
#define XW3 12
#define LDLY 16
#define FPUB 36
#define FX2 128
#define ABL 0
#define AMAP 1
#define XW5 10
#define P3REL 2
constexpr float QSCALE = 0.22360679774997896f * 1.4426950408889634f;

#define MFMA32(a, b, c) __builtin_amdgcn_mfma_f32_32x32x16_bf16((a), (b), (c), 0, 0, 0)
#define MFMA16(a, b, c) __builtin_amdgcn_mfma_f32_16x16x32_bf16((a), (b), (c), 0, 0, 0)
#define BAR() asm volatile("s_waitcnt lgkmcnt(0)\n\ts_barrier" ::: "memory")
#define SCHED() __builtin_amdgcn_sched_barrier(0)
#define RAWBAR() do { SCHED(); asm volatile("s_barrier" ::: "memory"); SCHED(); } while (0)
#define OPAQUE(v) asm volatile("" : "+v"(v))
#define SGB(m, n) __builtin_amdgcn_sched_group_barrier((m), (n), 0)
#define WAITV(n) asm volatile("s_waitcnt vmcnt(%0)" ::"i"(n) : "memory")

#define STAMP(i) do {} while (0)
#define STAMP2(i) do {} while (0)
#define STAMP_DUMP() do {} while (0)

DI void glds(unsigned voff, const char* sbase, unsigned ldst) {
  unsigned sv;
  asm volatile("s_mov_b32 %0, m0\n\ts_mov_b32 m0, %3\n\ts_nop 2\n\tglobal_load_lds_dwordx4 %1, %2\n\ts_mov_b32 m0, %0"
               : "=&s"(sv) : "v"(voff), "s"(sbase), "s"(ldst) : "memory");
}
DI void glds_sc1(unsigned voff, const char* sbase, unsigned ldst) {
  unsigned sv;
  asm volatile("s_mov_b32 %0, m0\n\ts_mov_b32 m0, %3\n\ts_nop 2\n\tglobal_load_lds_dwordx4 %1, %2 sc1\n\ts_mov_b32 m0, %0"
               : "=&s"(sv) : "v"(voff), "s"(sbase), "s"(ldst) : "memory");
}
DI void glds5_sc1(unsigned voff, const char* sbase, unsigned ldst) {
  unsigned sv;
  asm volatile("s_mov_b32 %0, m0\n\ts_mov_b32 m0, %3\n\ts_nop 2\n\t"
               "global_load_lds_dwordx4 %1, %2 offset:-2048 sc1\n\tglobal_load_lds_dwordx4 %1, %2 offset:-1024 sc1\n\t"
               "global_load_lds_dwordx4 %1, %2 sc1\n\tglobal_load_lds_dwordx4 %1, %2 offset:1024 sc1\n\t"
               "global_load_lds_dwordx4 %1, %2 offset:2048 sc1\n\ts_mov_b32 m0, %0"
               : "=&s"(sv) : "v"(voff), "s"(sbase + 2048), "s"(ldst + 2048) : "memory");
}
DI void glds5(unsigned voff, const char* sbase, unsigned ldst) {
  unsigned sv;
  asm volatile("s_mov_b32 %0, m0\n\ts_mov_b32 m0, %3\n\ts_nop 2\n\t"
               "global_load_lds_dwordx4 %1, %2 offset:-2048\n\tglobal_load_lds_dwordx4 %1, %2 offset:-1024\n\t"
               "global_load_lds_dwordx4 %1, %2\n\tglobal_load_lds_dwordx4 %1, %2 offset:1024\n\t"
               "global_load_lds_dwordx4 %1, %2 offset:2048\n\ts_mov_b32 m0, %0"
               : "=&s"(sv) : "v"(voff), "s"(sbase + 2048), "s"(ldst + 2048) : "memory");
}
DI void glds8(const unsigned (&v)[8], const char* sbase, unsigned ldst) {
  unsigned sv;
  asm volatile("s_mov_b32 %0, m0\n\ts_mov_b32 m0, %10\n\ts_nop 2\n\t"
               "global_load_lds_dwordx4 %1, %9 offset:-4096\n\tglobal_load_lds_dwordx4 %2, %9 offset:-3072\n\t"
               "global_load_lds_dwordx4 %3, %9 offset:-2048\n\tglobal_load_lds_dwordx4 %4, %9 offset:-1024\n\t"
               "global_load_lds_dwordx4 %5, %9\n\tglobal_load_lds_dwordx4 %6, %9 offset:1024\n\t"
               "global_load_lds_dwordx4 %7, %9 offset:2048\n\tglobal_load_lds_dwordx4 %8, %9 offset:3072\n\ts_mov_b32 m0, %0"
               : "=&s"(sv) : "v"(v[0]), "v"(v[1]), "v"(v[2]), "v"(v[3]), "v"(v[4]), "v"(v[5]), "v"(v[6]), "v"(v[7]),
                 "s"(sbase), "s"(ldst + 4096) : "memory");
}
DI void glds10_sc1(unsigned voff, const char* sbase, unsigned ldst) {
  unsigned sv;
  asm volatile("s_mov_b32 %0, m0\n\ts_mov_b32 m0, %4\n\ts_nop 2\n\t"
               "global_load_lds_dwordx4 %1, %3 offset:-4096 sc1\n\tglobal_load_lds_dwordx4 %1, %3 offset:-3072 sc1\n\t"
               "global_load_lds_dwordx4 %1, %3 offset:-2048 sc1\n\tglobal_load_lds_dwordx4 %1, %3 offset:-1024 sc1\n\t"
               "global_load_lds_dwordx4 %1, %3 sc1\n\tglobal_load_lds_dwordx4 %1, %3 offset:1024 sc1\n\t"
               "global_load_lds_dwordx4 %1, %3 offset:2048 sc1\n\tglobal_load_lds_dwordx4 %1, %3 offset:3072 sc1\n\t"
               "s_add_u32 m0, m0, 0x1000\n\ts_nop 0\n\t"
               "global_load_lds_dwordx4 %2, %3 sc1\n\tglobal_load_lds_dwordx4 %2, %3 offset:1024 sc1\n\ts_mov_b32 m0, %0"
               : "=&s"(sv) : "v"(voff), "v"(voff + 4096), "s"(sbase + 4096), "s"(ldst + 4096) : "memory", "scc");
}
DI void glds2_sc1(unsigned voff, const char* sbase, unsigned ldst) {
  unsigned sv;
  asm volatile("s_mov_b32 %0, m0\n\ts_mov_b32 m0, %3\n\ts_nop 2\n\t"
               "global_load_lds_dwordx4 %1, %2 sc1\n\tglobal_load_lds_dwordx4 %1, %2 offset:1024 sc1\n\ts_mov_b32 m0, %0"
               : "=&s"(sv) : "v"(voff), "s"(sbase), "s"(ldst) : "memory");
}
#define ST128(v, voff, soff) __builtin_amdgcn_raw_buffer_store_b128((v), rs, (unsigned)(voff) + (unsigned)(soff), 0, 16)
#define ST64(v, voff, soff) __builtin_amdgcn_raw_buffer_store_b64((v), rs, (unsigned)(voff) + (unsigned)(soff), 0, 16)
#define LD128(voff, soff) __builtin_amdgcn_raw_buffer_load_b128(rs, (unsigned)(voff), (unsigned)(soff), 16)
#define LD64(voff, soff) __builtin_amdgcn_raw_buffer_load_b64(rs, (unsigned)(voff), (unsigned)(soff), 16)
DI bf16x8 ld_b128(LAS char* p) { return *(LAS bf16x8*)p; }
DI s16x4 ld_tr(LAS char* p) { return __builtin_amdgcn_ds_read_tr16_b64_v4i16((LAS s16x4*)p); }
DI bf16x8 ld_tr2(LAS char* p0, LAS char* p1) {
  s16x4 a = ld_tr(p0), b = ld_tr(p1);
  return __builtin_shufflevector(a, b, 0, 1, 2, 3, 4, 5, 6, 7);
}
DI unsigned pk2(float lo, float hi) {
  f32x2 v = {lo, hi};
  return __builtin_bit_cast(unsigned, __builtin_convertvector(v, bf16x2));
}
DI float bflo(unsigned w) { return __uint_as_float(w << 16); }
DI float bfhi(unsigned w) { return __uint_as_float(w & 0xffff0000u); }
DI float sum32(float v) {
  auto r = __builtin_amdgcn_permlane32_swap(__float_as_uint(v), __float_as_uint(v), false, false);
  return __uint_as_float(r[0]) + __uint_as_float(r[1]);
}
DI float sum16(float v) {
  auto r = __builtin_amdgcn_permlane16_swap(__float_as_uint(v), __float_as_uint(v), false, false);
  return __uint_as_float(r[0]) + __uint_as_float(r[1]);
}
DI float sum8(float v) {
  v += __uint_as_float(__builtin_amdgcn_update_dpp(0, __float_as_uint(v), 0xB1, 0xf, 0xf, true));
  v += __uint_as_float(__builtin_amdgcn_update_dpp(0, __float_as_uint(v), 0x4E, 0xf, 0xf, true));
  v += __uint_as_float(__builtin_amdgcn_update_dpp(0, __float_as_uint(v), 0x141, 0xf, 0xf, true));
  return v;
}
DI float max32(float v) {
  auto r = __builtin_amdgcn_permlane32_swap(__float_as_uint(v), __float_as_uint(v), false, false);
  return fmaxf(__uint_as_float(r[0]), __uint_as_float(r[1]));
}
DI u64 mix64(u64 z) {
  z += 0x9E3779B97F4A7C15ull; z = (z ^ (z >> 30)) * 0xBF58476D1CE4E5B9ull; z = (z ^ (z >> 27)) * 0x94D049BB133111EBull;
  return z ^ (z >> 31);
}
DI u64 ld_flag(const char* p) { return __hip_atomic_load((const u64*)p, __ATOMIC_RELAXED, __HIP_MEMORY_SCOPE_AGENT); }
DI void st_flag(char* p, u64 v) { __hip_atomic_store((u64*)p, v, __ATOMIC_RELAXED, __HIP_MEMORY_SCOPE_AGENT); }
DI void wait_flags_from(const char* p, u64 v, u64 epoch) {
  for (unsigned spins = 0; spins < (1u << 18); ++spins) {
    if (__all(v == epoch)) break;
    __builtin_amdgcn_s_sleep(PSLP);
    v = ld_flag(p);
  }
  asm volatile("" ::: "memory");
}
DI void wait_flags(const char* p, u64 epoch) { wait_flags_from(p, ld_flag(p), epoch); }
DI f32x16 mfma32_c(bf16x8 a, bf16x8 b, const f32x16& c) {
  f32x16 d;
  asm volatile("s_nop 1\n\tv_mfma_f32_32x32x16_bf16 %0, %1, %2, %3" : "=&v"(d) : "v"(a), "v"(b), "v"(c));
  return d;
}

DI float dot2(unsigned a, unsigned b, float c) {
  return __builtin_amdgcn_fdot2_f32_bf16(__builtin_bit_cast(bf16x2, a), __builtin_bit_cast(bf16x2, b), c, false);
}
DI int rfl(int v) { return __builtin_amdgcn_readfirstlane(v); }
DI int mini(int a, int b) { return a < b ? a : b; }
typedef __attribute__((ext_vector_type(2))) int i32x2;
typedef __attribute__((ext_vector_type(4))) int i32x4;
template <int I> struct IC { static constexpr int value = I; };
template <int I, int N, class F> DI void static_for(F&& f) { if constexpr (I < N) { f(IC<I>{}); static_for<I + 1, N>(f); } }
#define FENCE() asm volatile("" ::: "memory")
constexpr int SPIN_CAP = 1 << 22;
DI void lds_wait_eq(unsigned v, unsigned addr, unsigned want) {
  unsigned s0, cnt;
  asm volatile("s_mov_b32 %2, 0x400000\n"
               "1:\n\tv_readfirstlane_b32 %1, %0\n\ts_cmp_eq_u32 %1, %4\n\ts_cbranch_scc1 2f\n\t"
               "ds_read_b32 %0, %3\n\ts_sub_u32 %2, %2, 1\n\ts_waitcnt lgkmcnt(0)\n\ts_cbranch_scc0 1b\n"
               "2:"
               : "+v"(v), "=&s"(s0), "=&s"(cnt) : "v"(addr), "s"(want) : "memory", "scc");
}
DI void lds_wait_ge(unsigned addr, unsigned need) {
  unsigned v, s0, cnt;
  asm volatile("s_mov_b32 %2, 0x400000\n"
               "1:\n\tds_read_b32 %0, %3\n\ts_waitcnt lgkmcnt(0)\n\tv_readfirstlane_b32 %1, %0\n\ts_cmp_ge_u32 %1, %4\n\ts_cbranch_scc1 2f\n\t"
               "s_sub_u32 %2, %2, 1\n\ts_cbranch_scc0 1b\n"
               "2:"
               : "=&v"(v), "=&s"(s0), "=&s"(cnt) : "v"(addr), "s"(need) : "memory", "scc");
}
DI void lds_wait_halves(unsigned v, unsigned addr, unsigned need) {
  unsigned s0, s1, cnt;
  asm volatile("s_mov_b32 %3, 0x400000\n"
               "1:\n\tv_readfirstlane_b32 %1, %0\n\ts_lshr_b32 %2, %1, 16\n\ts_and_b32 %1, %1, 0xffff\n\ts_min_u32 %1, %1, %2\n\t"
               "s_cmp_ge_u32 %1, %5\n\ts_cbranch_scc1 2f\n\t"
               "ds_read_b32 %0, %4\n\ts_sub_u32 %3, %3, 1\n\ts_waitcnt lgkmcnt(0)\n\ts_cbranch_scc0 1b\n"
               "2:"
               : "+v"(v), "=&s"(s0), "=&s"(s1), "=&s"(cnt) : "v"(addr), "s"(need) : "memory", "scc");
}

__global__ __launch_bounds__(512, 2) void fused_block(const char* __restrict__ x, const char* __restrict__ wqkv, const char* __restrict__ wo,
                                                      const char* __restrict__ wg, const char* __restrict__ wu, const char* __restrict__ wd,
                                                      char* __restrict__ out, char* ws) {
  __shared__ __attribute__((aligned(16))) char smem_[163840];
  LAS char* sm = (LAS char*)smem_;
  const unsigned sm0 = (unsigned)(uintptr_t)sm;
  const int tid = threadIdx.x, lane = tid & 63;
  const int wid = __builtin_amdgcn_readfirstlane(tid >> 6);
  STAMP(0);
  const int b = blockIdx.x, g = 4 * ((b & 7) >> 1) + (b >> 6), c = 8 * (b & 1) + ((b >> 3) & 7);
  const int wgid = g * 16 + c;
  const int g2 = AMAP == 0 ? g : AMAP == 1 ? (b >> 4) : 8 * (b & 1) + (b >> 5), c2 = AMAP == 0 ? c : AMAP == 1 ? 2 * (b & 7) + ((b >> 3) & 1) : 4 * ((b & 7) >> 1) + ((b >> 3) & 3);
  const int c1 = ((g & 1) && (c & 7) == 7) ? c ^ 8 : c;
  const bool lx = !(wid & 1);
  unsigned vo[8];
  {
    const unsigned rsh = lx ? 3 : 4, rstr = lx ? 1280 : 3840, pstr = (lx ? 8 * 1280 : 4 * 3840) - 1024;
    const unsigned cm = lx ? 7 : 15, csh = lx ? 0 : 2, chx = lx ? 64 : 0;
    const unsigned ch0 = (lane & cm) ^ ((lane >> 4) << csh);
    const unsigned vb = (lane >> rsh) * rstr + 4096;
#pragma unroll
    for (int i = 0; i < 8; ++i) vo[i] = vb + i * pstr + ((ch0 << 4) ^ ((i & 1) ? chx : 0));
  }
  const char* const rsb = (lx ? x : wqkv) + (lx ? g * (128 * 1280) : c1 * 256);
  const unsigned hstep = lx ? 64 * 1280 : 32 * 3840, sstep = lx ? 128 : 64 * 3840, rdst = sm0 + L_P1 + (wid & 1) * 16384;
  if (c1 < 15 && wid < 4) {
#pragma unroll
    for (int s = 0; s < 2; ++s) glds8(vo, rsb + s * sstep + (wid >> 1) * hstep, rdst + s * 32768 + (wid >> 1) * 8192);
  }
  SCHED();
  const auto rs = __builtin_amdgcn_make_buffer_rsrc(ws, 0, WS_END, 0x00020000);
  const bool isC = wid < 4, isL = (wid >> 1) == 2;
  const int l31 = lane & 31, hi = lane >> 5, G = lane >> 4, q4 = (lane & 15) >> 2, p4 = lane & 3, l15 = lane & 15;
  STAMP(6);
  u64 eraw = 0, epoch = 0;
  auto load_epoch = [&]() { unsigned z = 0; OPAQUE(z); eraw = ld_flag(ws + WS_EPOCH + z); };
  auto get_epoch = [&]() {
    const u64 e = mix64(eraw);
    const unsigned e0 = __builtin_amdgcn_readfirstlane((unsigned)e), e1 = __builtin_amdgcn_readfirstlane((unsigned)(e >> 32));
    epoch = ((u64)e1 << 32) | e0;
  };
  auto wset = [&](int w, int v) { FENCE(); *(volatile LAS int*)(sm + L_W + w) = v; FENCE(); };
  auto wget = [&](int w) { return rfl(*(volatile LAS int*)(sm + L_W + w)); };
  auto wget4 = [&](int w) { const i32x4 v = *(volatile LAS i32x4*)(sm + L_W + w); return rfl(mini(mini(v[0], v[1]), mini(v[2], v[3]))); };
  auto wwait = [&](int w, int need) { for (int sp = 0; wget(w) < need && sp < SPIN_CAP; ++sp) __builtin_amdgcn_s_sleep(1); FENCE(); };
  auto wraw = [&](int w) { return *(volatile LAS unsigned*)(sm + L_W + w); };
  auto wset16 = [&](int w, int v) { FENCE(); *(volatile LAS short*)(sm + L_W + w) = (short)v; FENCE(); };
  auto wwait2 = [&](int w, unsigned first, int need) { lds_wait_halves(first, sm0 + L_W + w, need); };
  auto arrive_n = [&](int k, unsigned flag_base, unsigned n) {
    if (lane == 0) {
      const unsigned old = __hip_atomic_fetch_add((LAS unsigned*)(sm + L_W + W_CNT + 4 * k), 1u, __ATOMIC_RELAXED, __HIP_MEMORY_SCOPE_WORKGROUP);
      if (old == n - 1) {
        st_flag(ws + flag_base + (g * 16 + c1) * 128, epoch);
        if (k == 1 && c1 == 14) st_flag(ws + WS_HINT + g * 128, epoch);
      }
    }
  };
  if (wid == 7) {
    u64 pc;
    asm volatile("s_getpc_b64 %0" : "=s"(pc));
    unsigned sv;
    const int ln = lane < CODE_LINES ? lane : 0;
    asm volatile("s_mov_b32 %0, m0\n\ts_mov_b32 m0, %3\n\ts_nop 2\n\tglobal_load_lds_dword %1, %2\n\ts_mov_b32 m0, %0"
                 : "=&s"(sv) : "v"((((b >> 3) & 7) * CODE_LINES + ln) * 128), "s"(pc), "s"(sm0 + L_JUNK) : "memory");
  }
  if (wid == 6) *(LAS unsigned*)(sm + L_W + 4 * lane) = 0;
  if (wid == 7) *(LAS u32x2*)(sm + L_ONES + 8 * lane) = u32x2{0x3f803f80u, 0x3f803f80u};
  if (wid == 6 && lane < 12) *(LAS u32x4*)(sm + L_KT + 16 * lane) = u32x4{lane < 8 - 4 * g2 ? 0x3f803f80u : 0x00003f80u, 0, 0, 0};

  auto arrive_w = [&](int k, unsigned flag_base) {
    if (lane == 0) {
      const unsigned old = __hip_atomic_fetch_add((LAS unsigned*)(sm + L_W + W_CNT + 4 * k), 1u, __ATOMIC_RELAXED, __HIP_MEMORY_SCOPE_WORKGROUP);
      if (old == 3) st_flag(ws + flag_base + wgid * 128, epoch);
    }
  };
  bf16x8 rf[10];
  unsigned rvo = 0, rvo1 = 0, rstep5 = 0;
  int rn = 0, roct = 0;
  auto wide_read = [&](unsigned st, unsigned dst, unsigned nk5) {
    LAS char* p = sm + st + q4 * 1280 + G * 320;
#pragma unroll
    for (int j = 0; j < 10; ++j) {
      const int tt = j % 5;
      LAS char* pj = p + (j / 5) * 160 + (tt < 4 ? 64 * (tt >> 1) + 16 * p4 + 8 * (tt & 1) : 128 + 8 * p4);
      rf[j] = ld_tr2(pj, pj + 4 * 1280);
    }
    rvo = rvo1 = dst + G * 2 * nk5 * 1024 + l15 * 16; rstep5 = nk5 * 1024; rn = 10;
  };
  auto retile_store = [&](int j) {
    const int o = roct + (j >> 1);
    ST128(__builtin_bit_cast(u32x4, rf[j]), (j & 1) ? rvo1 : rvo,
          rn == 10 ? (j % 5) * 1024 + (j / 5) * rstep5 : (o >> 1) * 3072 + (o & 1) * 512);
  };
  const int su = wgid * 3 + wid - 5;
  const unsigned rst = L_RP + (wid == 4 ? 0 : 10240 + (wid - 5) * 6144);
  auto late_load = [&]() {
    if (wid == 4) {
      if (wgid < 192) {
        glds5(lane * 16, wd + (size_t)wgid * 10240, sm0 + rst);
        glds5(lane * 16, wd + (size_t)wgid * 10240 + 5120, sm0 + rst + 5120);
      }
    } else if (su < 648) {
      const int cl = su % 24, rgp = su / 24;
      const unsigned vr = (lane >> 3) * 3072 + (lane & 7) * 16;
#pragma unroll
      for (int i = 0; i < 3; ++i) {
        const size_t so = (size_t)mini(3 * rgp + i, 79) * 8 * 3072 + cl * 128;
        glds(vr, wg + so, sm0 + rst + i * 1024);
        glds(vr, wu + so, sm0 + rst + 3072 + i * 1024);
      }
    }
  };
  auto late_read = [&]() {
    WAITV(0);
    if (wid == 4) {
      const int pos = 96 * ((wgid % 12) / 6) + 6 * (wgid / 12) + wgid % 6;
      if (wgid < 192) wide_read(rst, WS_W5 + (pos >> 2) * 5120 + (pos & 3) * 256, 240);
    } else if (su < 648) {
      const int cl = su % 24, rgp = su / 24;
      float cs[2] = {0.f, 0.f};
      rn = rgp == 26 ? 4 : 6;
#pragma unroll
      for (int j = 0; j < 6; ++j) {
        LAS char* p = sm + rst + (G & 1) * 3072 + ((j >> 1) * 8 + q4) * 128 + (2 * (j & 1) + hi) * 32 + 16 * (p4 & 1) + 8 * (p4 >> 1);
        rf[j] = ld_tr2(p, p + 4 * 128);
        const u32x4 w = __builtin_bit_cast(u32x4, rf[j]);
        const unsigned one = j < rn ? 0x3f803f80u : 0u;
#pragma unroll
        for (int d = 0; d < 4; ++d) cs[j & 1] = dot2(w[d], one, cs[j & 1]);
      }
      {
        const unsigned t0 = 4 * cl + hi, t1 = t0 + 2;
        rvo = WS_W4 + ((t0 / 3) * 120 + t0 % 3) * 1024 + l31 * 16;
        rvo1 = WS_W4 + ((t1 / 3) * 120 + t1 % 3) * 1024 + l31 * 16;
      }
      roct = 3 * rgp;
      const unsigned cvo = lane * 4;
#pragma unroll
      for (int jp = 0; jp < 2; ++jp)
        __builtin_amdgcn_raw_buffer_store_b32(__float_as_uint(cs[jp]), rs, cvo + WS_CSUM + (unsigned)(rgp * 12288 + cl * 512 + jp * 256), 0, 16);
    }
    FENCE();
    if (lane == 0) __hip_atomic_fetch_add((LAS unsigned*)(sm + L_W + W_S1DONE), 1u, __ATOMIC_RELAXED, __HIP_MEMORY_SCOPE_WORKGROUP);
    FENCE();
  };
  if (isL && c1 < 15) {
    unsigned vx[8], vw[8];
#pragma unroll
    for (int i = 0; i < 8; ++i) {
      vx[i] = (lane >> 3) * 1280 + 4096 + i * (8 * 1280 - 1024) + ((((lane & 7) ^ (lane >> 4)) << 4) ^ ((i & 1) * 64));
      vw[i] = (lane >> 4) * 3840 + 4096 + i * (4 * 3840 - 1024) + (((lane & 15) ^ ((lane >> 4) << 2)) << 4);
    }
    int s = 2 + (wid & 1);
    const char* px = x + g * (128 * 1280) + s * 128;
    const char* pw = wqkv + c1 * 256 + s * (64 * 3840);
    BAR();
#pragma unroll 1
    for (; s < 10; s += 2, px += 256, pw += 128 * 3840) {
      for (int sp = 0; wget4(W_CONS1) < s - 3 && sp < SPIN_CAP; ++sp) {}
      FENCE();
      const unsigned d = sm0 + L_P1 + (s & 3) * 32768;
      glds8(vx, px, d);
      glds8(vw, pw, d + 16384);
      WAITV(16);
      if (s >= 4) wset(W_INIT + 4 * (s - 2), 0x01010101);
      STAMP(16 + s - 2);
      glds8(vx, px + 64 * 1280, d + 8192);
      glds8(vw, pw + 32 * 3840, d + 24576);
    }
    WAITV(0);
    wset(W_INIT + 4 * (s - 2), 0x01010101);
    STAMP(24);
    load_epoch();
    late_load();
    get_epoch();
    late_read();
  } else if (isC && c1 < 15) {
    const int wn = wid & 1, wt = wid >> 1;
    f32x16 acc[2][2] = {};
    const unsigned ax = L_P1 + (64 * wt + l31) * 128 + ((hi ^ ((l31 >> 1) & 7)) << 4);
    const unsigned aw = L_P1 + 16384 + (8 * hi + q4) * 256 + (((8 * wn + 2 * (G & 1) + (p4 & 1)) ^ (q4 << 2)) << 4) + 8 * (p4 >> 1);
    bf16x8 xf[4][2], wf[4][2];
    auto rd = [&](int ks, unsigned so) {
#pragma unroll
      for (int t = 0; t < 2; ++t) {
        wf[ks][t] = ld_tr2(sm + (((aw ^ (t * 64)) + so) + ks * 4096), sm + (((aw ^ (t * 64)) + so) + ks * 4096 + 1024));
        xf[ks][t] = ld_b128(sm + (((ax ^ (ks * 32)) + so) + t * 4096));
      }
    };
    auto wset8 = [&](int w) { FENCE(); *(volatile LAS char*)(sm + L_W + w) = 1; FENCE(); };
    BAR();
    WAITV(8);
    wset8(W_INIT + wid);
    lds_wait_eq(0u, sm0 + L_W + W_INIT, 0x01010101u);
    STAMP(16);
    rd(0, 0); rd(1, 0);
    SCHED();
#pragma unroll 1
    for (int s = 0; s < 10; ++s) {
      const unsigned so = (s & 3) * 32768, sn = ((s + 1) & 3) * 32768;
      unsigned lv = 0;
#pragma unroll
      for (int ks = 0; ks < 4; ++ks) {
        const int wn1 = W_INIT + 4 * (s < 9 ? s + 1 : 9);
        if (ks == 0) lv = wraw(wn1);
        if (ks == 2) {
          if (s == 0) { WAITV(0); wset8(wn1 + wid); }
          lds_wait_eq(lv, sm0 + L_W + wn1, 0x01010101u);
          STAMP2(17 + s);
          SCHED();
        }
        if (ks < 2) rd(ks + 2, so); else rd(ks - 2, sn);
        if (ks == 1) wset(W_CONS1 + 4 * wid, s + 1);
#pragma unroll
        for (int tj = 0; tj < 2; ++tj)
#pragma unroll
          for (int ti = 0; ti < 2; ++ti) acc[tj][ti] = MFMA32(wf[ks][tj], xf[ks][ti], acc[tj][ti]);
        SGB(0x8, 1); SGB(0x100, 2); SGB(0x8, 1); SGB(0x100, 2); SGB(0x8, 1); SGB(0x100, 2); SGB(0x8, 1);
        SCHED();
      }
    }
    STAMP(1);
    load_epoch();
    wwait(W_STATS, 1);
    const float sc = (c1 < 5) ? QSCALE : 1.f;
#pragma unroll
    for (int ti = 0; ti < 2; ++ti) {
      const int tok = 64 * wt + 32 * ti + l31;
      const f32x2 st = *(LAS f32x2*)(sm + L_STAT + tok * 8);
      const float r = st[1] * sc, nm = -st[0];
#pragma unroll
      for (int tj = 0; tj < 2; ++tj)
#pragma unroll
        for (int hf = 0; hf < 2; ++hf) {
          const int col = 64 * wn + 32 * tj + 16 * hf + 8 * hi;
          const f32x4 ca = *(LAS f32x4*)(sm + L_CS + col * 4), cb = *(LAS f32x4*)(sm + L_CS + col * 4 + 16);
          float v[8];
#pragma unroll
          for (int j = 0; j < 4; ++j) {
            v[j] = (acc[tj][ti][8 * hf + j] + nm * ca[j]) * r;
            v[4 + j] = (acc[tj][ti][8 * hf + 4 + j] + nm * cb[j]) * r;
          }
          u32x4 o = {pk2(v[0], v[1]), pk2(v[2], v[3]), pk2(v[4], v[5]), pk2(v[6], v[7])};
          ST128(o, hi * 2048 + tok * 16, WS_QKV + (unsigned)(g * 240 + 16 * c1 + 8 * wn + 4 * tj + 2 * hf) * 2048);
        }
    }
    get_epoch();
    WAITV(0);
    arrive_n(1, WS_F1, 4);
  } else {
    const int sw = wid & 1;
    const bool srv = wid >= 6;
    load_epoch();
    u32x4 v[10];
    if (srv) {
      const char* src = sw ? wqkv + (size_t)(40 * g) * 3840 + (c1 < 15 ? c1 : 0) * 256 : x + (size_t)(128 * g + 8 * c1) * 1280;
      const unsigned vs = sw ? G * 3840 + l15 * 16 : (lane >> 3) * 1280 + (lane & 7) * 16, st = sw ? 4 * 3840 : 128;
#pragma unroll
      for (int i = 0; i < 10; ++i) v[i] = *(const u32x4*)(src + i * st + vs);
    }
    SCHED();
    BAR();
    if (srv) {
      STAMP(15);
      if (sw == 0) {
        float s = 0.f, q = 0.f;
#pragma unroll
        for (int i = 0; i < 10; ++i)
#pragma unroll
          for (int j = 0; j < 4; ++j) { s = dot2(v[i][j], 0x3f803f80u, s); q = dot2(v[i][j], v[i][j], q); }
        s = sum8(s); q = sum8(q);
        const float mu = s * (1.f / 640.f);
        const float rstd = __builtin_amdgcn_rsqf(fmaxf(q * (1.f / 640.f) - mu * mu, 0.f) + 1e-5f);
        if ((lane & 7) == 0) ST64((u32x2{__float_as_uint(mu), __float_as_uint(rstd)}), lane, WS_ST1 + (unsigned)(128 * g + 8 * c1) * 8);
      } else {
        float a[8] = {};
#pragma unroll
        for (int i = 0; i < 10; ++i)
#pragma unroll
          for (int j = 0; j < 4; ++j) { a[2 * j] += bflo(v[i][j]); a[2 * j + 1] += bfhi(v[i][j]); }
#pragma unroll
        for (int j = 0; j < 8; ++j) a[j] = sum32(sum16(a[j]));
        if (lane < 32) {
          const f32x4 o = (lane & 16) ? f32x4{a[4], a[5], a[6], a[7]} : f32x4{a[0], a[1], a[2], a[3]};
          ST128(__builtin_bit_cast(u32x4, o), l15 * 32 + (lane & 16), WS_CS1 + (unsigned)(c1 * 16 + g) * 512);
        }
      }
      get_epoch();
      WAITV(0);
      arrive_n(0, WS_F0, 2);
      STAMP(16);
    } else {
      get_epoch();
      if (wid < 5) {
        const int o = 5 * g + wid;
        const unsigned stg = L_HSTG + wid * 10240;
        glds5(lane * 16, wo + (size_t)o * 10240, sm0 + stg);
        glds5(lane * 16, wo + (size_t)o * 10240 + 5120, sm0 + stg + 5120);
        WAITV(0);
        wide_read(stg, WS_W3 + (o >> 2) * 5120 + (o & 3) * 256, 100);
#pragma unroll
        for (int j = 0; j < 10; ++j) retile_store(j);
        rn = 0;
        WAITV(0);
        arrive_n(1, WS_F1, 5);
      }
    }
    if (wid == 7 && c1 < 15) {
      __builtin_amdgcn_s_sleep(FDLY);
      wait_flags(ws + WS_F0 + (lane < 16 ? g * 16 + lane : (l15 * 16 + c1)) * 128, epoch);
      const u32x4 gs = LD128(lane * 16, WS_ST1 + (unsigned)g * 1024);
      f32x4 t = {};
#pragma unroll
      for (int i = 0; i < 8; ++i) t += __builtin_bit_cast(f32x4, LD128(hi * 4096 + l31 * 16, WS_CS1 + (unsigned)(c1 * 16 + i) * 512));
#pragma unroll
      for (int j = 0; j < 4; ++j) t[j] = sum32(t[j]);
      *(LAS u32x4*)(sm + L_STAT + lane * 16) = gs;
      if (lane < 32) *(LAS f32x4*)(sm + L_CS + lane * 16) = t;
      wset(W_STATS, 1);
      STAMP(19);
    }
    if (wid >= 4) { late_load(); late_read(); }
    STAMP(17);
    if (wid == 6) {
      if (c1 < 15) { for (int sp = 0; wget(W_CNT + 4) < 4 && sp < SPIN_CAP; ++sp) __builtin_amdgcn_s_sleep(1); __builtin_amdgcn_s_sleep(GDLY); }
      else wait_flags(ws + WS_HINT + g * 128, epoch);
      wwait(W_S1DONE, 4);
      const int i = lane < 18 ? lane : 0;
      const int which = i / 6, po = which ? (i % 6) >> 1 : 0, e = i & 1;
      const int tile = (640 * which + 40 * c2 + 39 * e) >> 7;
      const int pn = g2 - po < 0 ? 0 : g2 - po;
      wait_flags(ws + WS_F1 + (pn * 16 + tile) * 128, epoch);
      wset(W_GO, 1);
    }
  }
  const char* wsrc3 = ws + WS_W3 + (c & 7) * 102400 + (wid & 1) * 10240;
  const char* wsrc4 = ws + WS_W4 + c * 245760 + (wid & 1) * 10240 - 5 * 20480;
  const char* wsrc5 = ws + WS_W5 + (c & 7) * 245760 + (wid & 1) * 10240 - 17 * 20480;
  auto wissue_half = [&](int st, int h) {
    const char* sb = (st < 5 ? wsrc3 : st < 17 ? wsrc4 : wsrc5) + st * 20480 + h * 5120;
    glds5_sc1(lane * 16, sb, sm0 + L_RING + ((st + 3) % 7) * RSLOT + (wid & 1) * 10240 + h * 5120);
  };
  STAMP(2);

  bool wready;
  {
    const int hs = wid >> 2, qb = wid & 3;
    if (!isC) {
      const int bk = wid & 3, pn = (bk == 3 || g2 - bk < 0) ? g2 : g2 - bk;
      const char* sA = ws + WS_QKV + (size_t)(pn * 240 + (bk == 3 ? 0 : 80) + 5 * c2) * 2048;
      const unsigned dA = sm0 + (bk == 3 ? L_OST : L_K + (2 - bk) * 2048), dstr = bk == 3 ? 2048 : 6144, dB = sm0 + L_V + (2 - bk) * 2048;
      wwait(W_GO, 1);
      STAMP(3);
#pragma unroll
      for (int j = 0; j < 5; ++j) glds2_sc1(lane * 16, sA + j * 2048, dA + j * dstr);
      if (bk < 3) {
#pragma unroll
        for (int j = 0; j < 5; ++j) glds2_sc1(lane * 16, sA + (80 + j) * 2048, dB + j * VSTR);
      }
      if (wid == 7) {
        if (__all(ld_flag(ws + WS_F1 + (l15 * 16 + 15) * 128) == epoch)) wset(W_WREADY, 1);
      }
    }
    const unsigned ak0 = L_K + (3 * hs + hi) * 6144 + l31 * 16, ak1 = L_K + 2 * 6144 + l31 * 16;
    const int kb_lo = (g2 >= 2) ? qb : (g2 == 1 ? 4 : 8), kb_hi = qb + 8;
    const float NINF = -__builtin_inff();
    f32x16 cD, cF;
#pragma unroll
    for (int r = 0; r < 16; ++r) {
      const bool low = (r & 3) + 8 * (r >> 2) + 4 * hi <= l31;
      cD[r] = low ? 0.f : NINF;
      cF[r] = low ? NINF : 0.f;
    }
    const bool isOne = (G & 1) && p4;
    const unsigned av0 = isOne ? L_ONES
                               : L_V + (4 * hi + q4) * 16 + ((G & 1) ? 2 * VSTR + 8 * hs : (3 * hs + (p4 & 1)) * VSTR + 8 * (p4 >> 1));
    const unsigned avs = isOne ? 0 : 512;
    unsigned a0 = ak0 + qb * 512, a1 = hi ? L_KT + qb * 16 : ak1 + qb * 512, av = av0 + qb * avs;
    const unsigned a1s = hi ? 16 : 512;
    asm volatile("" : "+v"(cD), "+v"(cF), "+v"(a0), "+v"(a1), "+v"(av));
    if (wid >= 4 && wid < 7) WAITV(10); else WAITV(0);
    BAR();
    STAMP(4);
    wready = isL && wget(W_WREADY);
    const int row = 32 * qb + l31;
    u32x4 q0 = *(LAS u32x4*)(sm + L_OST + (3 * hs + hi) * 2048 + row * 16), q1 = *(LAS u32x4*)(sm + L_OST + 2 * 2048 + row * 16);
    if (hi || hs) { q1[0] = 0; q1[1] = 0; }
    if (hi || !hs) { q1[2] = 0; q1[3] = 0; }
    const bf16x8 qf0 = __builtin_bit_cast(bf16x8, q0), qf1 = __builtin_bit_cast(bf16x8, q1);
    auto vmax = [&](const f32x16& s, float m) {
#pragma unroll
      for (int r = 0; r < 16; r += 2) m = fmaxf(fmaxf(m, s[r]), s[r + 1]);
      return m;
    };
    f32x16 o;
    float rl, m;
    auto finish = [&]() {
      auto sw = __builtin_amdgcn_permlane32_swap(__float_as_uint(o[8]), __float_as_uint(o[8]), false, false);
      const float l = __uint_as_float(sw[1]);
      rl = __builtin_amdgcn_rcpf(l);
      float am = 0.f;
#pragma unroll
      for (int r = 0; r < 12; r += 2) am = fmaxf(fmaxf(am, fabsf(o[r])), fabsf(o[r + 1]));
      return (bool)__all(l <= 0x1p30f && l >= 0x1p-30f && am < __builtin_inff());
    };
    {
      f32x16 s[2];
      bf16x8 k0 = ld_b128(sm + a0 + 8 * 512), k1 = ld_b128(sm + a1 + 8 * a1s);
      s[1] = mfma32_c(k0, qf0, cD);
      s[0] = mfma32_c(k1, qf1, s[1]);
      asm volatile("s_nop 11" : "+v"(s[0]));
      m = max32(vmax(s[0], NINF));
      u32x4 q2 = q1;
      if (hi) q2[0] = pk2(-m, -3.0e38f);
      const bf16x8 qf2 = __builtin_bit_cast(bf16x8, q2);
      s[0] = MFMA32(k1, qf2, s[1]);
      k0 = ld_b128(sm + a0); k1 = ld_b128(sm + a1);
      o = f32x16{};
      SCHED();
      WAITV(0);
      BAR();
      SCHED();
#pragma unroll
      for (int j = 0; j < 9; ++j) {
        if (APRIO == 1) { if (((j + hs) & 1)) __builtin_amdgcn_s_setprio(1); else __builtin_amdgcn_s_setprio(0); }
        if (APRIO == 2 && j == 0 && hs) __builtin_amdgcn_s_setprio(1);
        if (APRIO == 2 && j == 5 && hs) __builtin_amdgcn_s_setprio(0);
        f32x16& sc = s[j & 1];
        f32x16& sn = s[(j + 1) & 1];
        unsigned avj = av + (j == 0 ? 8 : j - 1) * avs;
        bf16x8 vf0 = ld_tr2(sm + avj, sm + avj + 128), vf1 = ld_tr2(sm + avj + 256, sm + avj + 384);
        unsigned pw[8];
        if (j < 8) sn = MFMA32(k0, qf0, j == 0 ? cF : f32x16{});
        SCHED();
#pragma unroll
        for (int r = 0; r < 2; ++r) pw[r] = pk2(__builtin_amdgcn_exp2f(sc[2 * r]), __builtin_amdgcn_exp2f(sc[2 * r + 1]));
        SCHED();
        if (j < 8) sn = MFMA32(k1, qf2, sn);
        if (j < 7) { a1 += a1s; k0 = ld_b128(sm + a0 + (j + 1) * 512); k1 = ld_b128(sm + a1); }
        SCHED();
#pragma unroll
        for (int r = 2; r < 5; ++r) pw[r] = pk2(__builtin_amdgcn_exp2f(sc[2 * r]), __builtin_amdgcn_exp2f(sc[2 * r + 1]));
        SCHED();
        o = MFMA32(vf0, __builtin_bit_cast(bf16x8, (u32x4{pw[0], pw[1], pw[2], pw[3]})), o);
        SCHED();
#pragma unroll
        for (int r = 5; r < 8; ++r) pw[r] = pk2(__builtin_amdgcn_exp2f(sc[2 * r]), __builtin_amdgcn_exp2f(sc[2 * r + 1]));
        SCHED();
        o = MFMA32(vf1, __builtin_bit_cast(bf16x8, (u32x4{pw[4], pw[5], pw[6], pw[7]})), o);
        SCHED();
        if (j < 5 && 2 * j < rn) { retile_store(2 * j); retile_store(2 * j + 1); }
        if (j < 8 && wready) wissue_half(j >> 1, j & 1);
      }
    }
    if (APRIO) __builtin_amdgcn_s_setprio(0);
    if (!finish()) {
      int lq = l31 - 4 * hi;
      OPAQUE(lq);
#pragma unroll 1
      for (int k2 = kb_lo; k2 < kb_hi; ++k2) {
        f32x16 cz;
#pragma unroll
        for (int r = 0; r < 16; ++r) cz[r] = (k2 == qb && (r & 3) + 8 * (r >> 2) <= lq) ? NINF : 0.f;
        f32x16 s = MFMA32(ld_b128(sm + ak0 + k2 * 512), qf0, cz);
        s = MFMA32(ld_b128(sm + ak1 + k2 * 512), qf1, s);
        m = vmax(s, m);
      }
      m = max32(m);
      o = f32x16{};
#pragma unroll 1
      for (int kb = kb_lo; kb <= kb_hi; ++kb) {
        f32x16 cin;
#pragma unroll
        for (int r = 0; r < 16; ++r) cin[r] = ((r & 3) + 8 * (r >> 2) <= lq ? kb == qb : kb == kb_hi) ? NINF : -m;
        f32x16 s = MFMA32(ld_b128(sm + ak0 + kb * 512), qf0, cin);
        s = MFMA32(ld_b128(sm + ak1 + kb * 512), qf1, s);
        const unsigned av = av0 + kb * avs;
        bf16x8 vf0 = ld_tr2(sm + av, sm + av + 128), vf1 = ld_tr2(sm + av + 256, sm + av + 384);
        unsigned pw[8];
#pragma unroll
        for (int r = 0; r < 8; ++r) pw[r] = pk2(__builtin_amdgcn_exp2f(s[2 * r]), __builtin_amdgcn_exp2f(s[2 * r + 1]));
        o = MFMA32(vf0, __builtin_bit_cast(bf16x8, (u32x4{pw[0], pw[1], pw[2], pw[3]})), o);
        o = MFMA32(vf1, __builtin_bit_cast(bf16x8, (u32x4{pw[4], pw[5], pw[6], pw[7]})), o);
      }
      (void)finish();
    }
    const unsigned ob = WS_O + (unsigned)(g2 * 80 + 5 * c2) * 2048;
    ST128((u32x4{pk2(o[0] * rl, o[1] * rl), pk2(o[2] * rl, o[3] * rl), pk2(o[4] * rl, o[5] * rl), pk2(o[6] * rl, o[7] * rl)}),
          (3 * hs + hi) * 2048 + row * 16, ob);
    if (!hi) ST64((u32x2{pk2(o[8] * rl, o[9] * rl), pk2(o[10] * rl, o[11] * rl)}), row * 16 + 8 * hs, ob + 2 * 2048);
    STAMP(5);
    FENCE();
    if (lane == 0) __hip_atomic_fetch_add((LAS unsigned*)(sm + L_W + W_CNT + 8), 1u, __ATOMIC_RELAXED, __HIP_MEMORY_SCOPE_WORKGROUP);
    WAITV(0);
    if (lane == 0) {
      const unsigned old = __hip_atomic_fetch_add((LAS unsigned*)(sm + L_W + W_CNT + 12), 1u, __ATOMIC_RELAXED, __HIP_MEMORY_SCOPE_WORKGROUP);
      if (old == 7) { st_flag(ws + WS_F2 + (g2 * 16 + c2) * 128, epoch); st_flag(ws + WS_F1P + wgid * 8, epoch); }
    }
    STAMP(6);
    if (isC) wwait(W_CNT + 12, 8);
  }

  if (isL) {
    int issued = 4;
    if (!wready) {
#pragma unroll 1
      for (int i = 0; i < 1; ++i) wait_flags(ws + WS_F1 + (l15 * 16 + 15) * 128, epoch);
      issued = 0;
    }
    const int wl = W_LANDW + 2 * (wid & 1);
    if (wready) wset16(wl, 4);
    wwait(W_CNT + 8, 8);
    for (; issued < 5; ++issued) { const int st = rfl(issued); wissue_half(st, 0); wissue_half(st, 1); }
    WAITV(0);
    wset16(wl, 5);
    __builtin_amdgcn_s_sleep(LDLY);
    {
      u64 f[4];
#pragma unroll
      for (int i = 0; i < 4; ++i) f[i] = ld_flag(ws + WS_F1P + i * 512 + lane * 8);
#pragma unroll
      for (int i = 0; i < 4; ++i) wait_flags_from(ws + WS_F1P + i * 512 + lane * 8, f[i], epoch);
    }
    STAMP(30);
    if (wid == 4) wset(W_GO, 2);
    const char* p = wsrc4 + 5 * 20480;
    unsigned dst = sm0 + L_RING + RSLOT + (wid & 1) * 10240;
#pragma unroll 1
    for (int st = 5; st < 29; ++st) {
      if (st == 7 + P3REL) { WAITV(0); wset16(wl, st); }
      for (int sp = 0; wget4(W_CONSW) < (st < 7 ? 1 : st - 5) && sp < SPIN_CAP; ++sp) __builtin_amdgcn_s_sleep(1);
      FENCE();
      glds10_sc1(lane * 16, p, dst);
      p = st == 16 ? wsrc5 + 17 * 20480 : p + 20480;
      dst = dst + RSLOT >= sm0 + L_RING + 7 * RSLOT ? dst - 6 * RSLOT : dst + RSLOT;
      WAITV(20);
      if (st >= 7 && (st < 7 + P3REL || st > 8 + P3REL)) wset16(wl, st - 1);
      STAMP(31 + st - 1);
    }
    WAITV(10);
    wset16(wl, 28);
    WAITV(0);
    wset16(wl, 29);
    STAMP(60);
    STAMP_DUMP();
    return;
  }
  if (!isC) {
    if (wid == 6) {
#pragma unroll 1
      for (int k = 0; k < 2; ++k) {
        for (int sp = 0; (k < 1 ? wget4(W_DONE) <= k : wget(W_CNT + 20) < 4) && sp < SPIN_CAP; ++sp) __builtin_amdgcn_s_sleep(2);
        __builtin_amdgcn_s_sleep(PDLY);
        wait_flags(ws + WS_F4 + k * FL + (g * 16 + l15) * 128, epoch);
        wset(W_RDY, k + 3);
        STAMP(22 + k);
      }
    } else {
      for (int sp = 0; wget(W_GO) < 2 && sp < SPIN_CAP; ++sp) __builtin_amdgcn_s_sleep(4);
      FENCE();
      if (lane < 48) {
        f32x4 t = {};
#pragma unroll
        for (int i = 0; i < 27; ++i) t += __builtin_bit_cast(f32x4, LD128(lane * 16, WS_CSUM + (unsigned)(i * 12288 + c * 768)));
#pragma unroll
        for (int e = 0; e < 4; ++e) {
          const unsigned ch = pk2(t[e], t[e]);
          *(LAS u32x4*)(sm + L_WX + lane * 64 + e * 16) = u32x4{ch, pk2(t[e] - bflo(ch), 0.f), 0, 0};
        }
      }
      wset(W_WREADY, 2);
#pragma unroll 1
      for (int k = 0; k < 1; ++k) {
        for (int sp = 0; wget4(W_DONE) <= k && sp < SPIN_CAP; ++sp) __builtin_amdgcn_s_sleep(1);
        FENCE();
        if (lane == 0) st_flag(ws + WS_F4 + k * FL + wgid * 128, epoch);
        STAMP(22 + k);
      }
      if (b == 0) {
#pragma unroll 1
        for (int i = 0; i < 4; ++i) wait_flags(ws + WS_F2 + (i * 64 + lane) * 128, epoch);
        if (lane == 0) st_flag(ws + WS_EPOCH, epoch);
      }
    }
    STAMP_DUMP();
    return;
  }

  const int wdn = W_DONE + 4 * wid;
  const int wc = W_CONSW + 4 * wid;
  const unsigned lane16 = lane * 16;

  const int r3 = 64 * (c >> 3) + 16 * wid + l15;
  {
    unsigned hpk[10];
    const char* xp = x + (size_t)(128 * g + r3) * 1280 + (c & 7) * 160;
    const u32x4 xr0 = *(const u32x4*)(xp + G * 16), xr1 = *(const u32x4*)(xp + 64 + G * 16);
    const u32x2 xr2 = *(const u32x2*)(xp + 128 + G * 8);
    u32x4 xf[20];

    f32x4 acc[5] = {};
    {
      constexpr int NB = 15;
      bf16x8 wf[NB];
      unsigned so = 0, lv = 0;
      auto rd = [&](int f) {
        if (f == 80) wwait2(W_LANDW, lv, 5);
        if (f % 20 == 0) { so = lane16; OPAQUE(so); so += slot_of(f / 20); OPAQUE(so); }
        wf[f % NB] = ld_b128(sm + so + (f % 20) * 1024);
        if (f == 64) lv = wraw(W_LANDW);
      };
      __builtin_amdgcn_s_sleep(PDLY - 4);
      wwait2(W_LANDW, 0u, 4);
#pragma unroll
      for (int f = 0; f < NB; ++f) rd(f);
      SCHED();
      wait_flags(ws + WS_F2 + (g * 16 + l15) * 128, epoch);
      STAMP(7);
#pragma unroll
      for (int s = 0; s < XW3; ++s) xf[s] = LD128(G * 2048 + r3 * 16, WS_O + (unsigned)(g * 80 + 4 * s) * 2048);
      SCHED();
#pragma unroll
      for (int f = 0; f < 100; ++f) {
        acc[f % 5] = MFMA16(wf[f % NB], __builtin_bit_cast(bf16x8, xf[f / 5]), acc[f % 5]);
        SCHED();
        if (f + NB < 100) rd(f + NB);
        if (f % 3 == 0 && XW3 + f / 3 < 20) xf[XW3 + f / 3] = LD128(G * 2048 + r3 * 16, WS_O + (unsigned)(g * 80 + 4 * (XW3 + f / 3)) * 2048);
        static_assert(3 * (20 - XW3) + NB >= 20 * P3REL - 1, "released stages are read");
        if (f == 3 * (20 - XW3)) wset(wc, 1 + P3REL);
        SCHED();
      }
    }
    STAMP(8);
    float s1 = 0.f, s2 = 0.f;
#pragma unroll
    for (int d = 0; d < 10; ++d) {
      const unsigned xv = d < 4 ? xr0[d & 3] : d < 8 ? xr1[d & 3] : xr2[d & 1];
      const unsigned a = pk2(acc[d >> 1][2 * (d & 1)], acc[d >> 1][2 * (d & 1) + 1]);
      const unsigned h = pk2(bflo(a) + bflo(xv), bfhi(a) + bfhi(xv));
      hpk[d] = h;
      s1 += bflo(h) + bfhi(h);
      s2 += bflo(h) * bflo(h) + bfhi(h) * bfhi(h);
    }
    const unsigned hb = WS_H + (unsigned)(g * 80 + 10 * (c & 7)) * 2048;
    ST128((u32x4{hpk[0], hpk[1], hpk[2], hpk[3]}), G * 2048 + r3 * 16, hb);
    ST128((u32x4{hpk[4], hpk[5], hpk[6], hpk[7]}), G * 2048 + r3 * 16, hb + 4 * 2048);
    {
      auto a0 = __builtin_amdgcn_permlane16_swap(hpk[8], hpk[8], false, false);
      auto a1 = __builtin_amdgcn_permlane16_swap(hpk[9], hpk[9], false, false);
      if (!(G & 1)) ST128((u32x4{a0[0], a1[0], a0[1], a1[1]}), (G >> 1) * 2048 + r3 * 16, hb + 8 * 2048);
    }
    s1 = sum32(sum16(s1)); s2 = sum32(sum16(s2));
    if (G == 0) ST64((u32x2{__float_as_uint(s1), __float_as_uint(s2)}), r3 * 8, WS_STAT + (unsigned)(g * 8 + (c & 7)) * 1024);
    *(LAS u32x4*)(sm + L_OST + wid * 2560 + lane * 16) = u32x4{hpk[0], hpk[1], hpk[2], hpk[3]};
    *(LAS u32x4*)(sm + L_OST + wid * 2560 + 1024 + lane * 16) = u32x4{hpk[4], hpk[5], hpk[6], hpk[7]};
    *(LAS u32x2*)(sm + L_OST + wid * 2560 + 2048 + lane * 8) = u32x2{hpk[8], hpk[9]};
    WAITV(0);
    arrive_w(4, WS_F3);
    STAMP(9);
  }

  {
    constexpr int NB = 6;
    bf16x8 wf[NB];
    unsigned so = 0;
    unsigned lv = 0;
    auto rd = [&](int n) __attribute__((always_inline)) {
      const int part = n / 123, i = n % 123, f = 120 * part + i;
      if (i >= 120) {
        if (n == 120) lds_wait_ge(sm0 + L_W + W_WREADY, 2);
        wf[n % NB] = ld_b128(sm + L_WX + (3 * part + i - 120) * 512 + (lane16 & 511));
      } else {
      if (f % 20 == 0) { wwait2(W_LANDW, lv, 5 + f / 20 + 1); so = lane16; OPAQUE(so); so += slot_of(5 + f / 20); OPAQUE(so); }
      wf[n % NB] = ld_b128(sm + so + (f % 20) * 1024);
      if (f % 20 == 12) lv = wraw(W_LANDW);
      if (f % 20 == 19) wset(wc, 5 + f / 20 + 2);
      }
    };
    __builtin_amdgcn_s_sleep(PDLY - 2);
#pragma unroll
    for (int n = 0; n < NB; ++n) rd(n);
    SCHED();
    wait_flags(ws + WS_F3 + (g * 16 + l15) * 128, epoch);
    STAMP(10);
    const int r4 = 32 * wid + l31;
    const unsigned vx = hi * 2048 + r4 * 16;
    const unsigned hb = WS_H + (unsigned)(g * 80) * 2048;
    u32x4 xf[40];
    u32x2 stp[4];
    constexpr int XW = 10;
#pragma unroll
    for (int i = 0; i < XW; ++i) xf[i] = LD128(vx, hb + i * 4096);
    SCHED();
    float r2 = 0.f;
    unsigned xe0 = 0, xe1 = 0;
    static_for<0, 2>([&](auto pc) __attribute__((always_inline)) {
      constexpr int part = pc.value;
      f32x16 acc[3] = {};
      static_for<0, 123>([&](auto ic) __attribute__((always_inline)) {
        constexpr int i = ic.value;
        const int n = 123 * part + i;
        if (i % 20 == 0 && n > 0 && i < 120) { STAMP2(42 + (120 * part + i) / 20); SCHED(); }
        acc[i % 3] = MFMA32(wf[n % NB], __builtin_bit_cast(bf16x8, i < 120 ? xf[i < 120 ? i / 3 : 0] : u32x4{xe0, xe1, 0, 0}), acc[i % 3]);
        SCHED();
        if (n + NB < 246) rd(n + NB);
        if (part == 0 && (i & 1) && (i >> 1) + XW < 40) xf[(i >> 1) + XW] = LD128(vx, hb + ((i >> 1) + XW) * 4096);
        if (n >= 4 && n < 12 && !(n & 1)) stp[(n - 4) >> 1] = LD64(hi * 4096 + r4 * 8, WS_STAT + (unsigned)(g * 8 + ((n - 4) >> 1)) * 1024);
        if (n == 44) {
          float s1 = 0.f, s2 = 0.f;
#pragma unroll
          for (int j = 0; j < 4; ++j) { s1 += __uint_as_float(stp[j][0]); s2 += __uint_as_float(stp[j][1]); }
          s1 = sum32(s1) * (1.f / 640.f); s2 = sum32(s2) * (1.f / 640.f);
          r2 = __builtin_amdgcn_rcpf(fmaxf(s2 - s1 * s1, 0.f) + 1e-5f);
          const unsigned mh = pk2(-s1, 0.f);
          xe0 = hi ? 0u : pk2(-s1, -s1 - bflo(mh));
          xe1 = hi ? 0u : mh;
          OPAQUE(r2); OPAQUE(xe0); OPAQUE(xe1);
        }
        SCHED();
        if (part == 1 && i == 36) { WAITV(0); wset(wdn, 1); SCHED(); }
      });
      STAMP2(54 + part);
      if (part == 1) STAMP(11);
#pragma unroll
      for (int t = 0; t < 3; ++t) {
        unsigned o[4];
#pragma unroll
        for (int d = 0; d < 4; ++d) {
          const f32x2 hv = (f32x2{acc[t][2 * d], acc[t][2 * d + 1]} * r2) * f32x2{acc[t][8 + 2 * d], acc[t][8 + 2 * d + 1]};
          o[d] = pk2(hv[0], hv[1]);
        }
        ST128((u32x4{o[0], o[1], o[2], o[3]}), vx, WS_HH + (unsigned)(g * 192 + 96 * part + 6 * c + 2 * t) * 2048);
      }
      SCHED();
    });
    STAMP(12);
  }

  {
    unsigned ln5 = lane16;
    OPAQUE(ln5);
    const int wk = wid & 1;
    const unsigned hb5 = WS_HH + (unsigned)(g * 192) * 2048 + wk * 16384;
    const unsigned v50 = (ln5 >> 8) * 2048 + (64 * (c >> 3) + 16 * wid) * 16 + (ln5 & 255), v51 = v50 ^ 256;
    f32x4 acc[5] = {}, acp[5] = {};
    constexpr int NB = 8;
    bf16x8 wf[NB];
    u32x4 x5[24];
    unsigned so = 0;
    unsigned lv = 0;
    auto rd = [&](int e) {
      if (e % 10 == 0) { wwait2(W_LANDW, lv, 17 + e / 10 + 1); so = ln5 + wk * 10240; OPAQUE(so); so += slot_of(17 + e / 10); OPAQUE(so); }
      wf[e % NB] = ld_b128(sm + so + (e % 10) * 1024);
      if (e % 10 == 3) lv = wraw(W_LANDW);
      if (e % 10 == 9) wset(wc, 17 + e / 10 + 2);
    };
    auto ldx = [&](int part, int j) {
      const int kk = 12 * part + (j >> 1);
      x5[j] = LD128((j & 1) ? v51 : v50, hb5 + (4 * (kk >> 1) + (kk & 1)) * 8192);
    };
    lds_wait_ge(sm0 + L_W + W_RDY, 3);
    STAMP(40);
#pragma unroll
    for (int j = 0; j < XW5; ++j) ldx(0, j);
#pragma unroll
    for (int e = 0; e < NB; ++e) rd(e);
    SCHED();
    WAITV(XW5);
    arrive_w(5, WS_F4 + FL);
    STAMP(13);
    SCHED();
#pragma unroll
    for (int part = 0; part < 2; ++part) {
#pragma unroll
      for (int i = 0; i < 60; ++i) {
        const int e = 60 * part + i;
        acc[i % 5] = MFMA16(wf[e % NB], __builtin_bit_cast(bf16x8, x5[2 * (i / 5)]), acc[i % 5]);
        SCHED();
        acp[i % 5] = MFMA16(wf[e % NB], __builtin_bit_cast(bf16x8, x5[2 * (i / 5) + 1]), acp[i % 5]);
        SCHED();
        if (e + NB < 120) rd(e + NB);
        if (XW5 + i < 24) ldx(part, XW5 + i);
        SCHED();
      }
      if (part == 0) {
        lds_wait_ge(sm0 + L_W + W_RDY, 4);
        STAMP(41);
#pragma unroll
        for (int j = 0; j < XW5; ++j) ldx(1, j);
        SCHED();
      }
    }
    for (int sp = 0; wget4(W_CONSW) < 24 && sp < SPIN_CAP; ++sp) {}
    FENCE();
#pragma unroll
    for (int tt = 0; tt < 5; ++tt) *(LAS f32x4*)(sm + slot_of(22) + (wid ^ 1) * 5120 + tt * 1024 + ln5) = acp[tt];
    wset(wdn, 9);
    lds_wait_ge(sm0 + L_W + W_DONE + 4 * (wid ^ 1), 9);
#pragma unroll
    for (int tt = 0; tt < 5; ++tt) acc[tt] += *(LAS f32x4*)(sm + slot_of(22) + wid * 5120 + tt * 1024 + ln5);
    STAMP(14);
    unsigned opk[10];
    const u32x4 h0 = *(LAS u32x4*)(sm + L_OST + wid * 2560 + ln5), h1 = *(LAS u32x4*)(sm + L_OST + wid * 2560 + 1024 + ln5);
    const u32x2 h2 = *(LAS u32x2*)(sm + L_OST + wid * 2560 + 2048 + (ln5 >> 1));
#pragma unroll
    for (int d = 0; d < 10; ++d) {
      const unsigned hv = d < 4 ? h0[d & 3] : d < 8 ? h1[d & 3] : h2[d & 1];
      const unsigned a = pk2(acc[d >> 1][2 * (d & 1)], acc[d >> 1][2 * (d & 1) + 1]);
      opk[d] = pk2(bflo(a) + bflo(hv), bfhi(a) + bfhi(hv));
    }
    const unsigned G5 = ln5 >> 8;
    char* op = out + (size_t)(128 * g + 64 * (c >> 3) + 16 * wid) * 1280 + (c & 7) * 160 + (ln5 & 255) * 80;
    *(u32x4*)(op + G5 * 16) = u32x4{opk[0], opk[1], opk[2], opk[3]};
    *(u32x4*)(op + 64 + G5 * 16) = u32x4{opk[4], opk[5], opk[6], opk[7]};
    *(u32x2*)(op + 128 + G5 * 8) = u32x2{opk[8], opk[9]};
    STAMP(15);
    STAMP_DUMP();
  }
}

extern "C" void kernel_launch(void* const* d_in, const int* in_sizes, int n_in, void* d_out, int out_size, void* d_ws, size_t ws_size,
                              hipStream_t stream) {
  fused_block<<<256, 512, 0, stream>>>((const char*)d_in[0], (const char*)d_in[1], (const char*)d_in[2], (const char*)d_in[4],
                                       (const char*)d_in[5], (const char*)d_in[6], (char*)d_out, (char*)d_ws);
}
```

```cpp
#include <hip/hip_runtime.h>
#include <stdint.h>

typedef unsigned long long u64;
typedef __attribute__((ext_vector_type(8))) short bf16x8;
typedef __attribute__((ext_vector_type(4))) short s16x4;
typedef __attribute__((ext_vector_type(2))) __bf16 bf16x2;
typedef __attribute__((ext_vector_type(16))) float f32x16;
typedef __attribute__((ext_vector_type(4))) float f32x4;
typedef __attribute__((ext_vector_type(2))) float f32x2;
typedef __attribute__((ext_vector_type(4))) unsigned u32x4;
typedef __attribute__((ext_vector_type(2))) unsigned u32x2;
#define LAS __attribute__((address_space(3)))
#define DI __device__ __forceinline__

constexpr unsigned WS_EPOCH = 0, WS_HINT = 1024;
constexpr unsigned FL = 256 * 128;
constexpr unsigned WS_F0 = 4096, WS_F1 = WS_F0 + FL, WS_F2 = WS_F1 + FL, WS_F3 = WS_F2 + FL, WS_F4 = WS_F3 + FL;
constexpr unsigned WS_F1P = WS_F4 + 3 * FL;
constexpr unsigned WS_CSUM = WS_F1P + 2048;
constexpr unsigned WS_ST1 = WS_CSUM + 27 * 12288;
constexpr unsigned WS_CS1 = WS_ST1 + 2048 * 8;
constexpr unsigned WS_STAT = WS_CS1 + 16 * 16 * 512;
constexpr unsigned WS_QKV = 1u << 20;
constexpr unsigned WS_O = WS_QKV + 16 * 240 * 2048;
constexpr unsigned WS_H = WS_O + 16 * 80 * 2048;
constexpr unsigned WS_HH = WS_H + 16 * 80 * 2048;
constexpr unsigned WS_W3 = WS_HH + 16 * 192 * 2048;
constexpr unsigned WS_W4 = WS_W3 + 8 * 100 * 1024;
constexpr unsigned WS_W5 = WS_W4 + 16 * 240 * 1024;
constexpr unsigned WS_END = WS_W5 + 8 * 240 * 1024;
constexpr unsigned WS_DIAG = 32u << 20;
static_assert(WS_STAT + 16 * 8 * 1024 <= WS_QKV && WS_END <= WS_DIAG, "workspace map");

constexpr unsigned L_STAT = 0, L_CS = 1024;
constexpr unsigned L_W = 1536;
constexpr unsigned L_ONES = 1792;
constexpr unsigned L_KT = 2304;
constexpr unsigned L_RP = 2560;
constexpr unsigned L_OST = 2560;
constexpr unsigned L_JUNK = 31488;
constexpr unsigned L_HSTG = 81920;
constexpr unsigned L_WX = 12800;
constexpr int CODE_LINES = 48;
constexpr unsigned L_P1 = 32768;
constexpr unsigned L_K = 12800, L_V = L_K + 5 * 6144, VSTR = 6208;
constexpr unsigned L_RING = 20480, RSLOT = 20480;
constexpr unsigned slot_of(int i) { return L_RING + ((i + 3) % 7) * RSLOT; }
static_assert(L_V + 5 * VSTR <= L_RING + 3 * RSLOT, "LDS map");
enum { W_LAND1 = 0, W_LANDW = 8, W_CONS1 = 16, W_CONSW = 32, W_GO = 48, W_S1DONE = 52, W_STATS = 56, W_WREADY = 60, W_RDY = 64, W_DONE = 80, W_CNT = 96, W_INIT = 128, W_EPO = 176, W_LL = 184 };

#define P4REL 0
#define APRIO 2
#define PDLY 24
#define PSLP 1
#define GDLY 16
#define FDLY 24
#define XW3 12
#define LDLY 16
#define FPUB 36
#define FX2 128
#define ABL 0
#define AMAP 1
#define XW5 10
#define P3REL 2
#define EV 3
#define LP 0
#define NTS " nt"
#define NT1 ""
#define NT2 0
#define NT3 0
#define NT5 0
#define NT6 0
#define NT4 0
constexpr float QSCALE = 0.22360679774997896f * 1.4426950408889634f;

#define MFMA32(a, b, c) __builtin_amdgcn_mfma_f32_32x32x16_bf16((a), (b), (c), 0, 0, 0)
#define MFMA16(a, b, c) __builtin_amdgcn_mfma_f32_16x16x32_bf16((a), (b), (c), 0, 0, 0)
#define BAR() asm volatile("s_waitcnt lgkmcnt(0)\n\ts_barrier" ::: "memory")
#define SCHED() __builtin_amdgcn_sched_barrier(0)
#define RAWBAR() do { SCHED(); asm volatile("s_barrier" ::: "memory"); SCHED(); } while (0)
#define OPAQUE(v) asm volatile("" : "+v"(v))
#define SGB(m, n) __builtin_amdgcn_sched_group_barrier((m), (n), 0)
#define WAITV(n) asm volatile("s_waitcnt vmcnt(%0)" ::"i"(n) : "memory")

#define STAMP(i) do {} while (0)
#define STAMP2(i) do {} while (0)
#define STAMP_DUMP() do {} while (0)

DI void glds(unsigned voff, const char* sbase, unsigned ldst) {
  unsigned sv;
  asm volatile("s_mov_b32 %0, m0\n\ts_mov_b32 m0, %3\n\ts_nop 2\n\tglobal_load_lds_dwordx4 %1, %2" NTS "\n\ts_mov_b32 m0, %0"
               : "=&s"(sv) : "v"(voff), "s"(sbase), "s"(ldst) : "memory");
}
DI void glds_sc1(unsigned voff, const char* sbase, unsigned ldst) {
  unsigned sv;
  asm volatile("s_mov_b32 %0, m0\n\ts_mov_b32 m0, %3\n\ts_nop 2\n\tglobal_load_lds_dwordx4 %1, %2 sc1\n\ts_mov_b32 m0, %0"
               : "=&s"(sv) : "v"(voff), "s"(sbase), "s"(ldst) : "memory");
}
DI void glds5_sc1(unsigned voff, const char* sbase, unsigned ldst) {
  unsigned sv;
  asm volatile("s_mov_b32 %0, m0\n\ts_mov_b32 m0, %3\n\ts_nop 2\n\t"
               "global_load_lds_dwordx4 %1, %2 offset:-2048 sc1\n\tglobal_load_lds_dwordx4 %1, %2 offset:-1024 sc1\n\t"
               "global_load_lds_dwordx4 %1, %2 sc1\n\tglobal_load_lds_dwordx4 %1, %2 offset:1024 sc1\n\t"
               "global_load_lds_dwordx4 %1, %2 offset:2048 sc1\n\ts_mov_b32 m0, %0"
               : "=&s"(sv) : "v"(voff), "s"(sbase + 2048), "s"(ldst + 2048) : "memory");
}
DI void glds5(unsigned voff, const char* sbase, unsigned ldst) {
  unsigned sv;
  asm volatile("s_mov_b32 %0, m0\n\ts_mov_b32 m0, %3\n\ts_nop 2\n\t"
               "global_load_lds_dwordx4 %1, %2 offset:-2048" NTS "\n\tglobal_load_lds_dwordx4 %1, %2 offset:-1024" NTS "\n\t"
               "global_load_lds_dwordx4 %1, %2" NTS "\n\tglobal_load_lds_dwordx4 %1, %2 offset:1024" NTS "\n\t"
               "global_load_lds_dwordx4 %1, %2 offset:2048" NTS "\n\ts_mov_b32 m0, %0"
               : "=&s"(sv) : "v"(voff), "s"(sbase + 2048), "s"(ldst + 2048) : "memory");
}
DI void glds8(const unsigned (&v)[8], const char* sbase, unsigned ldst) {
  unsigned sv;
  asm volatile("s_mov_b32 %0, m0\n\ts_mov_b32 m0, %10\n\ts_nop 2\n\t"
               "global_load_lds_dwordx4 %1, %9 offset:-4096" NT1 "\n\tglobal_load_lds_dwordx4 %2, %9 offset:-3072" NT1 "\n\t"
               "global_load_lds_dwordx4 %3, %9 offset:-2048" NT1 "\n\tglobal_load_lds_dwordx4 %4, %9 offset:-1024" NT1 "\n\t"
               "global_load_lds_dwordx4 %5, %9" NT1 "\n\tglobal_load_lds_dwordx4 %6, %9 offset:1024" NT1 "\n\t"
               "global_load_lds_dwordx4 %7, %9 offset:2048" NT1 "\n\tglobal_load_lds_dwordx4 %8, %9 offset:3072" NT1 "\n\ts_mov_b32 m0, %0"
               : "=&s"(sv) : "v"(v[0]), "v"(v[1]), "v"(v[2]), "v"(v[3]), "v"(v[4]), "v"(v[5]), "v"(v[6]), "v"(v[7]),
                 "s"(sbase), "s"(ldst + 4096) : "memory");
}
DI void glds10_sc1(unsigned voff, const char* sbase, unsigned ldst) {
  unsigned sv;
  asm volatile("s_mov_b32 %0, m0\n\ts_mov_b32 m0, %4\n\ts_nop 2\n\t"
               "global_load_lds_dwordx4 %1, %3 offset:-4096 sc1\n\tglobal_load_lds_dwordx4 %1, %3 offset:-3072 sc1\n\t"
               "global_load_lds_dwordx4 %1, %3 offset:-2048 sc1\n\tglobal_load_lds_dwordx4 %1, %3 offset:-1024 sc1\n\t"
               "global_load_lds_dwordx4 %1, %3 sc1\n\tglobal_load_lds_dwordx4 %1, %3 offset:1024 sc1\n\t"
               "global_load_lds_dwordx4 %1, %3 offset:2048 sc1\n\tglobal_load_lds_dwordx4 %1, %3 offset:3072 sc1\n\t"
               "s_add_u32 m0, m0, 0x1000\n\ts_nop 0\n\t"
               "global_load_lds_dwordx4 %2, %3 sc1\n\tglobal_load_lds_dwordx4 %2, %3 offset:1024 sc1\n\ts_mov_b32 m0, %0"
               : "=&s"(sv) : "v"(voff), "v"(voff + 4096), "s"(sbase + 4096), "s"(ldst + 4096) : "memory", "scc");
}
DI void glds2_sc1(unsigned voff, const char* sbase, unsigned ldst) {
  unsigned sv;
  asm volatile("s_mov_b32 %0, m0\n\ts_mov_b32 m0, %3\n\ts_nop 2\n\t"
               "global_load_lds_dwordx4 %1, %2 sc1\n\tglobal_load_lds_dwordx4 %1, %2 offset:1024 sc1\n\ts_mov_b32 m0, %0"
               : "=&s"(sv) : "v"(voff), "s"(sbase), "s"(ldst) : "memory");
}
#define ST128(v, voff, soff) __builtin_amdgcn_raw_buffer_store_b128((v), rs, (unsigned)(voff) + (unsigned)(soff), 0, 16 | NT4)
#define ST128R(v, voff, soff) __builtin_amdgcn_raw_buffer_store_b128((v), rs, (unsigned)(voff) + (unsigned)(soff), 0, 16 | NT3)
#define ST64(v, voff, soff) __builtin_amdgcn_raw_buffer_store_b64((v), rs, (unsigned)(voff) + (unsigned)(soff), 0, 16)
#define LD128(voff, soff) __builtin_amdgcn_raw_buffer_load_b128(rs, (unsigned)(voff), (unsigned)(soff), 16)
#define LD64(voff, soff) __builtin_amdgcn_raw_buffer_load_b64(rs, (unsigned)(voff), (unsigned)(soff), 16)
DI bf16x8 ld_b128(LAS char* p) { return *(LAS bf16x8*)p; }
DI s16x4 ld_tr(LAS char* p) { return __builtin_amdgcn_ds_read_tr16_b64_v4i16((LAS s16x4*)p); }
DI bf16x8 ld_tr2(LAS char* p0, LAS char* p1) {
  s16x4 a = ld_tr(p0), b = ld_tr(p1);
  return __builtin_shufflevector(a, b, 0, 1, 2, 3, 4, 5, 6, 7);
}
DI unsigned pk2(float lo, float hi) {
  f32x2 v = {lo, hi};
  return __builtin_bit_cast(unsigned, __builtin_convertvector(v, bf16x2));
}
DI float bflo(unsigned w) { return __uint_as_float(w << 16); }
DI float bfhi(unsigned w) { return __uint_as_float(w & 0xffff0000u); }
DI float sum32(float v) {
  auto r = __builtin_amdgcn_permlane32_swap(__float_as_uint(v), __float_as_uint(v), false, false);
  return __uint_as_float(r[0]) + __uint_as_float(r[1]);
}
DI float sum16(float v) {
  auto r = __builtin_amdgcn_permlane16_swap(__float_as_uint(v), __float_as_uint(v), false, false);
  return __uint_as_float(r[0]) + __uint_as_float(r[1]);
}
DI float sum8(float v) {
  v += __uint_as_float(__builtin_amdgcn_update_dpp(0, __float_as_uint(v), 0xB1, 0xf, 0xf, true));
  v += __uint_as_float(__builtin_amdgcn_update_dpp(0, __float_as_uint(v), 0x4E, 0xf, 0xf, true));
  v += __uint_as_float(__builtin_amdgcn_update_dpp(0, __float_as_uint(v), 0x141, 0xf, 0xf, true));
  return v;
}
DI float max32(float v) {
  auto r = __builtin_amdgcn_permlane32_swap(__float_as_uint(v), __float_as_uint(v), false, false);
  return fmaxf(__uint_as_float(r[0]), __uint_as_float(r[1]));
}
DI u64 mix64(u64 z) {
  z += 0x9E3779B97F4A7C15ull; z = (z ^ (z >> 30)) * 0xBF58476D1CE4E5B9ull; z = (z ^ (z >> 27)) * 0x94D049BB133111EBull;
  return z ^ (z >> 31);
}
DI u64 ld_flag(const char* p) { return __hip_atomic_load((const u64*)p, __ATOMIC_RELAXED, __HIP_MEMORY_SCOPE_AGENT); }
DI void st_flag(char* p, u64 v) { __hip_atomic_store((u64*)p, v, __ATOMIC_RELAXED, __HIP_MEMORY_SCOPE_AGENT); }
DI void wait_flags_from(const char* p, u64 v, u64 epoch) {
  for (unsigned spins = 0; spins < (1u << 18); ++spins) {
    if (__all(v == epoch)) break;
    __builtin_amdgcn_s_sleep(PSLP);
    v = ld_flag(p);
  }
  asm volatile("" ::: "memory");
}
DI void wait_flags(const char* p, u64 epoch) { wait_flags_from(p, ld_flag(p), epoch); }
DI f32x16 mfma32_c(bf16x8 a, bf16x8 b, const f32x16& c) {
  f32x16 d;
  asm volatile("s_nop 1\n\tv_mfma_f32_32x32x16_bf16 %0, %1, %2, %3" : "=&v"(d) : "v"(a), "v"(b), "v"(c));
  return d;
}

DI float dot2(unsigned a, unsigned b, float c) {
  return __builtin_amdgcn_fdot2_f32_bf16(__builtin_bit_cast(bf16x2, a), __builtin_bit_cast(bf16x2, b), c, false);
}
DI int rfl(int v) { return __builtin_amdgcn_readfirstlane(v); }
DI int mini(int a, int b) { return a < b ? a : b; }
typedef __attribute__((ext_vector_type(2))) int i32x2;
typedef __attribute__((ext_vector_type(4))) int i32x4;
template <int I> struct IC { static constexpr int value = I; };
template <int I, int N, class F> DI void static_for(F&& f) { if constexpr (I < N) { f(IC<I>{}); static_for<I + 1, N>(f); } }
#define FENCE() asm volatile("" ::: "memory")
constexpr int SPIN_CAP = 1 << 22;
DI void lds_wait_eq(unsigned v, unsigned addr, unsigned want) {
  unsigned s0, cnt;
  asm volatile("s_mov_b32 %2, 0x400000\n"
               "1:\n\tv_readfirstlane_b32 %1, %0\n\ts_cmp_eq_u32 %1, %4\n\ts_cbranch_scc1 2f\n\t"
               "ds_read_b32 %0, %3\n\ts_sub_u32 %2, %2, 1\n\ts_waitcnt lgkmcnt(0)\n\ts_cbranch_scc0 1b\n"
               "2:"
               : "+v"(v), "=&s"(s0), "=&s"(cnt) : "v"(addr), "s"(want) : "memory", "scc");
}
DI void lds_wait_ge(unsigned addr, unsigned need) {
  unsigned v, s0, cnt;
  asm volatile("s_mov_b32 %2, 0x400000\n"
               "1:\n\tds_read_b32 %0, %3\n\ts_waitcnt lgkmcnt(0)\n\tv_readfirstlane_b32 %1, %0\n\ts_cmp_ge_u32 %1, %4\n\ts_cbranch_scc1 2f\n\t"
               "s_sub_u32 %2, %2, 1\n\ts_cbranch_scc0 1b\n"
               "2:"
               : "=&v"(v), "=&s"(s0), "=&s"(cnt) : "v"(addr), "s"(need) : "memory", "scc");
}
DI void lds_wait_halves(unsigned v, unsigned addr, unsigned need) {
  unsigned s0, s1, cnt;
  asm volatile("s_mov_b32 %3, 0x400000\n"
               "1:\n\tv_readfirstlane_b32 %1, %0\n\ts_lshr_b32 %2, %1, 16\n\ts_and_b32 %1, %1, 0xffff\n\ts_min_u32 %1, %1, %2\n\t"
               "s_cmp_ge_u32 %1, %5\n\ts_cbranch_scc1 2f\n\t"
               "ds_read_b32 %0, %4\n\ts_sub_u32 %3, %3, 1\n\ts_waitcnt lgkmcnt(0)\n\ts_cbranch_scc0 1b\n"
               "2:"
               : "+v"(v), "=&s"(s0), "=&s"(s1), "=&s"(cnt) : "v"(addr), "s"(need) : "memory", "scc");
}

__global__ __launch_bounds__(512, 2) void fused_block(const char* __restrict__ x, const char* __restrict__ wqkv, const char* __restrict__ wo,
                                                      const char* __restrict__ wg, const char* __restrict__ wu, const char* __restrict__ wd,
                                                      char* __restrict__ out, char* ws) {
  __shared__ __attribute__((aligned(16))) char smem_[163840];
  LAS char* sm = (LAS char*)smem_;
  const unsigned sm0 = (unsigned)(uintptr_t)sm;
  const int tid = threadIdx.x, lane = tid & 63;
  const int wid = __builtin_amdgcn_readfirstlane(tid >> 6);
  STAMP(0);
  const int b = blockIdx.x, g = 4 * ((b & 7) >> 1) + (b >> 6), c = 8 * (b & 1) + ((b >> 3) & 7);
  const int wgid = g * 16 + c;
  const int g2 = AMAP == 0 ? g : AMAP == 1 ? (b >> 4) : 8 * (b & 1) + (b >> 5), c2 = AMAP == 0 ? c : AMAP == 1 ? 2 * (b & 7) + ((b >> 3) & 1) : 4 * ((b & 7) >> 1) + ((b >> 3) & 3);
  const int c1 = ((g & 1) && (c & 7) == 7) ? c ^ 8 : c;
  const bool lx = !(wid & 1);
  unsigned vo[8];
  {
    const unsigned rsh = lx ? 3 : 4, rstr = lx ? 1280 : 3840, pstr = (lx ? 8 * 1280 : 4 * 3840) - 1024;
    const unsigned cm = lx ? 7 : 15, csh = lx ? 0 : 2, chx = lx ? 64 : 0;
    const unsigned ch0 = (lane & cm) ^ ((lane >> 4) << csh);
    const unsigned vb = (lane >> rsh) * rstr + 4096;
#pragma unroll
    for (int i = 0; i < 8; ++i) vo[i] = vb + i * pstr + ((ch0 << 4) ^ ((i & 1) ? chx : 0));
  }
  const char* const rsb = (lx ? x : wqkv) + (lx ? g * (128 * 1280) : c1 * 256);
  const unsigned hstep = lx ? 64 * 1280 : 32 * 3840, sstep = lx ? 128 : 64 * 3840, rdst = sm0 + L_P1 + (wid & 1) * 16384;
  if (c1 < 15 && wid < 4) {
#pragma unroll
    for (int s = 0; s < 2; ++s) glds8(vo, rsb + s * sstep + (wid >> 1) * hstep, rdst + s * 32768 + (wid >> 1) * 8192);
  }
  SCHED();
  const auto rs = __builtin_amdgcn_make_buffer_rsrc(ws, 0, WS_END, 0x00020000);
  const bool isC = wid < 4, isL = (wid >> 1) == 2;
  const int l31 = lane & 31, hi = lane >> 5, G = lane >> 4, q4 = (lane & 15) >> 2, p4 = lane & 3, l15 = lane & 15;
  STAMP(6);
  u64 eraw = 0, epoch = 0;
  auto load_epoch = [&]() { unsigned z = 0; OPAQUE(z); eraw = ld_flag(ws + WS_EPOCH + z); };
  auto get_epoch = [&]() {
    const u64 e = mix64(eraw);
    const unsigned e0 = __builtin_amdgcn_readfirstlane((unsigned)e), e1 = __builtin_amdgcn_readfirstlane((unsigned)(e >> 32));
    epoch = ((u64)e1 << 32) | e0;
  };
  auto wset = [&](int w, int v) { FENCE(); *(volatile LAS int*)(sm + L_W + w) = v; FENCE(); };
  auto wget = [&](int w) { return rfl(*(volatile LAS int*)(sm + L_W + w)); };
  auto wget4 = [&](int w) { const i32x4 v = *(volatile LAS i32x4*)(sm + L_W + w); return rfl(mini(mini(v[0], v[1]), mini(v[2], v[3]))); };
  auto wwait = [&](int w, int need) { for (int sp = 0; wget(w) < need && sp < SPIN_CAP; ++sp) __builtin_amdgcn_s_sleep(1); FENCE(); };
  auto wraw = [&](int w) { return *(volatile LAS unsigned*)(sm + L_W + w); };
  auto wset16 = [&](int w, int v) { FENCE(); *(volatile LAS short*)(sm + L_W + w) = (short)v; FENCE(); };
  auto wwait2 = [&](int w, unsigned first, int need) { lds_wait_halves(first, sm0 + L_W + w, need); };
  auto arrive_n = [&](int k, unsigned flag_base, unsigned n) {
    if (lane == 0) {
      const unsigned old = __hip_atomic_fetch_add((LAS unsigned*)(sm + L_W + W_CNT + 4 * k), 1u, __ATOMIC_RELAXED, __HIP_MEMORY_SCOPE_WORKGROUP);
      if (old == n - 1) {
        st_flag(ws + flag_base + (g * 16 + c1) * 128, epoch);
        if (k == 1 && c1 == 14) st_flag(ws + WS_HINT + g * 128, epoch);
      }
    }
  };
  if (wid == 7) {
    u64 pc;
    asm volatile("s_getpc_b64 %0" : "=s"(pc));
    unsigned sv;
    const int ln = lane < CODE_LINES ? lane : 0;
    asm volatile("s_mov_b32 %0, m0\n\ts_mov_b32 m0, %3\n\ts_nop 2\n\tglobal_load_lds_dword %1, %2\n\ts_mov_b32 m0, %0"
                 : "=&s"(sv) : "v"((((b >> 3) & 7) * CODE_LINES + ln) * 128), "s"(pc), "s"(sm0 + L_JUNK) : "memory");
  }
  if (wid == 6) *(LAS unsigned*)(sm + L_W + 4 * lane) = 0;
  if (wid == 7) *(LAS u32x2*)(sm + L_ONES + 8 * lane) = u32x2{0x3f803f80u, 0x3f803f80u};
  if (wid == 6 && lane < 12) *(LAS u32x4*)(sm + L_KT + 16 * lane) = u32x4{lane < 8 - 4 * g2 ? 0x3f803f80u : 0x00003f80u, 0, 0, 0};

  auto arrive_w = [&](int k, unsigned flag_base) {
    if (lane == 0) {
      const unsigned old = __hip_atomic_fetch_add((LAS unsigned*)(sm + L_W + W_CNT + 4 * k), 1u, __ATOMIC_RELAXED, __HIP_MEMORY_SCOPE_WORKGROUP);
      if (old == 3) st_flag(ws + flag_base + wgid * 128, epoch);
    }
  };
  bf16x8 rf[10];
  unsigned rvo = 0, rvo1 = 0, rstep5 = 0;
  int rn = 0, roct = 0;
  auto wide_read = [&](unsigned st, unsigned dst, unsigned nk5) {
    LAS char* p = sm + st + q4 * 1280 + G * 320;
#pragma unroll
    for (int j = 0; j < 10; ++j) {
      const int tt = j % 5;
      LAS char* pj = p + (j / 5) * 160 + (tt < 4 ? 64 * (tt >> 1) + 16 * p4 + 8 * (tt & 1) : 128 + 8 * p4);
      rf[j] = ld_tr2(pj, pj + 4 * 1280);
    }
    rvo = rvo1 = dst + G * 2 * nk5 * 1024 + l15 * 16; rstep5 = nk5 * 1024; rn = 10;
  };
  auto retile_store = [&](int j) {
    const int o = roct + (j >> 1);
    ST128R(__builtin_bit_cast(u32x4, rf[j]), (j & 1) ? rvo1 : rvo,
          rn == 10 ? (j % 5) * 1024 + (j / 5) * rstep5 : (o >> 1) * 3072 + (o & 1) * 512);
  };
  const int su = wgid * 3 + wid - 5;
  const unsigned rst = L_RP + (wid == 4 ? 0 : 10240 + (wid - 5) * 6144);
  auto late_load = [&](int w) {
    const unsigned rsw = sm0 + L_RP + (w == 4 ? 0 : 10240 + (w - 5) * 6144);
    const int suw = wgid * 3 + w - 5;
    if (w == 4) {
      if (wgid < 192) {
        glds5(lane * 16, wd + (size_t)wgid * 10240, rsw);
        glds5(lane * 16, wd + (size_t)wgid * 10240 + 5120, rsw + 5120);
      }
    } else if (suw < 648) {
      const int cl = suw % 24, rgp = suw / 24;
      const unsigned vr = LP == 0 ? (lane >> 3) * 3072 + (lane & 7) * 16 : LP == 1 ? lane * 16 : (lane >> 5) * 3072 + (lane & 31) * 16;
#pragma unroll
      for (int i = 0; i < 3; ++i) {
        const size_t so = LP == 0 ? (size_t)mini(3 * rgp + i, 79) * 8 * 3072 + cl * 128 : LP == 1 ? (size_t)mini(suw, 639) * 3072 + i * 1024
                          : (size_t)(mini(wgid / 6, 34) * 18 + (w - 5) * 6 + 2 * i) * 3072 + (wgid % 6) * 512;
        glds(vr, wg + so, rsw + i * 1024);
        glds(vr, wu + so, rsw + 3072 + i * 1024);
      }
    }
  };
  auto late_read = [&]() {
    WAITV(0);
    if (wid == 4) {
      const int pos = 96 * ((wgid % 12) / 6) + 6 * (wgid / 12) + wgid % 6;
      if (wgid < 192) wide_read(rst, WS_W5 + (pos >> 2) * 5120 + (pos & 3) * 256, 240);
    } else if (su < 648) {
      const int cl = su % 24, rgp = su / 24;
      float cs[2] = {0.f, 0.f};
      rn = rgp == 26 ? 4 : 6;
#pragma unroll
      for (int j = 0; j < 6; ++j) {
        LAS char* p = sm + rst + (G & 1) * 3072 + ((j >> 1) * 8 + q4) * 128 + (2 * (j & 1) + hi) * 32 + 16 * (p4 & 1) + 8 * (p4 >> 1);
        rf[j] = ld_tr2(p, p + 4 * 128);
        const u32x4 w = __builtin_bit_cast(u32x4, rf[j]);
        const unsigned one = j < rn ? 0x3f803f80u : 0u;
#pragma unroll
        for (int d = 0; d < 4; ++d) cs[j & 1] = dot2(w[d], one, cs[j & 1]);
      }
      {
        const unsigned t0 = 4 * cl + hi, t1 = t0 + 2;
        rvo = WS_W4 + ((t0 / 3) * 120 + t0 % 3) * 1024 + l31 * 16;
        rvo1 = WS_W4 + ((t1 / 3) * 120 + t1 % 3) * 1024 + l31 * 16;
      }
      roct = 3 * rgp;
      const unsigned cvo = lane * 4;
#pragma unroll
      for (int jp = 0; jp < 2; ++jp)
        __builtin_amdgcn_raw_buffer_store_b32(__float_as_uint(cs[jp]), rs, cvo + WS_CSUM + (unsigned)(rgp * 12288 + cl * 512 + jp * 256), 0, 16);
    }
    FENCE();
    if (lane == 0) __hip_atomic_fetch_add((LAS unsigned*)(sm + L_W + W_S1DONE), 1u, __ATOMIC_RELAXED, __HIP_MEMORY_SCOPE_WORKGROUP);
    FENCE();
  };
  if (isL && c1 < 15) {
    unsigned vx[8], vw[8];
#pragma unroll
    for (int i = 0; i < 8; ++i) {
      vx[i] = (lane >> 3) * 1280 + 4096 + i * (8 * 1280 - 1024) + ((((lane & 7) ^ (lane >> 4)) << 4) ^ ((i & 1) * 64));
      vw[i] = (lane >> 4) * 3840 + 4096 + i * (4 * 3840 - 1024) + (((lane & 15) ^ ((lane >> 4) << 2)) << 4);
    }
    int s = 2 + (wid & 1);
    const char* px = x + g * (128 * 1280) + s * 128;
    const char* pw = wqkv + c1 * 256 + s * (64 * 3840);
    BAR();
#pragma unroll 1
    for (; s < 10; s += 2, px += 256, pw += 128 * 3840) {
      for (int sp = 0; wget4(W_CONS1) < s - 3 && sp < SPIN_CAP; ++sp) {}
      FENCE();
      const unsigned d = sm0 + L_P1 + (s & 3) * 32768;
      glds8(vx, px, d);
      glds8(vw, pw, d + 16384);
      WAITV(16);
      if (s >= 4) wset(W_INIT + 4 * (s - 2), 0x01010101);
      STAMP(16 + s - 2);
      glds8(vx, px + 64 * 1280, d + 8192);
      glds8(vw, pw + 32 * 3840, d + 24576);
    }
    WAITV(0);
    wset(W_INIT + 4 * (s - 2), 0x01010101);
    STAMP(24);
    load_epoch();
    if (wid == 4 ? !(EV & 4) : !(EV & 2)) late_load(wid);
    get_epoch();
    if (wid == 4 && (EV & 4)) wwait(W_STATS, 1);
    if (wid == 5 && (EV & 2)) wwait(W_LL, 1);
    late_read();
  } else if (isC && c1 < 15) {
    const int wn = wid & 1, wt = wid >> 1;
    f32x16 acc[2][2] = {};
    const unsigned ax = L_P1 + (64 * wt + l31) * 128 + ((hi ^ ((l31 >> 1) & 7)) << 4);
    const unsigned aw = L_P1 + 16384 + (8 * hi + q4) * 256 + (((8 * wn + 2 * (G & 1) + (p4 & 1)) ^ (q4 << 2)) << 4) + 8 * (p4 >> 1);
    bf16x8 xf[4][2], wf[4][2];
    auto rd = [&](int ks, unsigned so) {
#pragma unroll
      for (int t = 0; t < 2; ++t) {
        wf[ks][t] = ld_tr2(sm + (((aw ^ (t * 64)) + so) + ks * 4096), sm + (((aw ^ (t * 64)) + so) + ks * 4096 + 1024));
        xf[ks][t] = ld_b128(sm + (((ax ^ (ks * 32)) + so) + t * 4096));
      }
    };
    auto wset8 = [&](int w) { FENCE(); *(volatile LAS char*)(sm + L_W + w) = 1; FENCE(); };
    BAR();
    WAITV(8);
    wset8(W_INIT + wid);
    lds_wait_eq(0u, sm0 + L_W + W_INIT, 0x01010101u);
    STAMP(16);
    rd(0, 0); rd(1, 0);
    SCHED();
#pragma unroll 1
    for (int s = 0; s < 10; ++s) {
      const unsigned so = (s & 3) * 32768, sn = ((s + 1) & 3) * 32768;
      unsigned lv = 0;
#pragma unroll
      for (int ks = 0; ks < 4; ++ks) {
        const int wn1 = W_INIT + 4 * (s < 9 ? s + 1 : 9);
        if (ks == 0) lv = wraw(wn1);
        if (ks == 2) {
          if (s == 0) { WAITV(0); wset8(wn1 + wid); }
          lds_wait_eq(lv, sm0 + L_W + wn1, 0x01010101u);
          STAMP2(17 + s);
          SCHED();
        }
        if (ks < 2) rd(ks + 2, so); else rd(ks - 2, sn);
        if (ks == 1) wset(W_CONS1 + 4 * wid, s + 1);
#pragma unroll
        for (int tj = 0; tj < 2; ++tj)
#pragma unroll
          for (int ti = 0; ti < 2; ++ti) acc[tj][ti] = MFMA32(wf[ks][tj], xf[ks][ti], acc[tj][ti]);
        SGB(0x8, 1); SGB(0x100, 2); SGB(0x8, 1); SGB(0x100, 2); SGB(0x8, 1); SGB(0x100, 2); SGB(0x8, 1);
        SCHED();
      }
    }
    STAMP(1);
    wwait(W_STATS, 1);
    {
      const u32x2 e = *(volatile LAS u32x2*)(sm + L_W + W_EPO);
      epoch = ((u64)(unsigned)rfl(e[1]) << 32) | (unsigned)rfl(e[0]);
    }
    const float sc = (c1 < 5) ? QSCALE : 1.f;
#pragma unroll
    for (int ti = 0; ti < 2; ++ti) {
      const int tok = 64 * wt + 32 * ti + l31;
      const f32x2 st = *(LAS f32x2*)(sm + L_STAT + tok * 8);
      const float r = st[1] * sc, nm = -st[0];
#pragma unroll
      for (int tj = 0; tj < 2; ++tj)
#pragma unroll
        for (int hf = 0; hf < 2; ++hf) {
          const int col = 64 * wn + 32 * tj + 16 * hf + 8 * hi;
          const f32x4 ca = *(LAS f32x4*)(sm + L_CS + col * 4), cb = *(LAS f32x4*)(sm + L_CS + col * 4 + 16);
          float v[8];
#pragma unroll
          for (int j = 0; j < 4; ++j) {
            v[j] = (acc[tj][ti][8 * hf + j] + nm * ca[j]) * r;
            v[4 + j] = (acc[tj][ti][8 * hf + 4 + j] + nm * cb[j]) * r;
          }
          u32x4 o = {pk2(v[0], v[1]), pk2(v[2], v[3]), pk2(v[4], v[5]), pk2(v[6], v[7])};
          ST128(o, hi * 2048 + tok * 16, WS_QKV + (unsigned)(g * 240 + 16 * c1 + 8 * wn + 4 * tj + 2 * hf) * 2048);
        }
    }
    WAITV(0);
    arrive_n(1, WS_F1, 4);
  } else {
    const int sw = wid & 1;
    const bool srv = wid >= 6;
    load_epoch();
    u32x4 v[10];
    if (srv) {
      const char* src = sw ? wqkv + (size_t)(40 * g) * 3840 + (c1 < 15 ? c1 : 0) * 256 : x + (size_t)(128 * g + 8 * c1) * 1280;
      const unsigned vs = sw ? G * 3840 + l15 * 16 : (lane >> 3) * 1280 + (lane & 7) * 16, st = sw ? 4 * 3840 : 128;
#pragma unroll
      for (int i = 0; i < 10; ++i) v[i] = NT2 ? __builtin_nontemporal_load((const u32x4*)(src + i * st + vs)) : *(const u32x4*)(src + i * st + vs);
    }
    SCHED();
    BAR();
    if (srv) {
      STAMP(15);
      if (sw == 0) {
        float s = 0.f, q = 0.f;
#pragma unroll
        for (int i = 0; i < 10; ++i)
#pragma unroll
          for (int j = 0; j < 4; ++j) { s = dot2(v[i][j], 0x3f803f80u, s); q = dot2(v[i][j], v[i][j], q); }
        s = sum8(s); q = sum8(q);
        const float mu = s * (1.f / 640.f);
        const float rstd = __builtin_amdgcn_rsqf(fmaxf(q * (1.f / 640.f) - mu * mu, 0.f) + 1e-5f);
        if ((lane & 7) == 0) ST64((u32x2{__float_as_uint(mu), __float_as_uint(rstd)}), lane, WS_ST1 + (unsigned)(128 * g + 8 * c1) * 8);
      } else {
        float a[8] = {};
#pragma unroll
        for (int i = 0; i < 10; ++i)
#pragma unroll
          for (int j = 0; j < 4; ++j) { a[2 * j] += bflo(v[i][j]); a[2 * j + 1] += bfhi(v[i][j]); }
#pragma unroll
        for (int j = 0; j < 8; ++j) a[j] = sum32(sum16(a[j]));
        if (lane < 32) {
          const f32x4 o = (lane & 16) ? f32x4{a[4], a[5], a[6], a[7]} : f32x4{a[0], a[1], a[2], a[3]};
          __builtin_amdgcn_raw_buffer_store_b128(__builtin_bit_cast(u32x4, o), rs, l15 * 32 + (lane & 16) + WS_CS1 + (unsigned)(c1 * 16 + g) * 512, 0, 16 | NT6);
        }
      }
      get_epoch();
      WAITV(0);
      arrive_n(0, WS_F0, 2);
      STAMP(16);
    } else {
      get_epoch();
      if (wid < 5) {
        const int o = 5 * g + wid;
        const unsigned stg = L_HSTG + wid * 10240;
        glds5(lane * 16, wo + (size_t)o * 10240, sm0 + stg);
        glds5(lane * 16, wo + (size_t)o * 10240 + 5120, sm0 + stg + 5120);
        WAITV(0);
        wide_read(stg, WS_W3 + (o >> 2) * 5120 + (o & 3) * 256, 100);
#pragma unroll
        for (int j = 0; j < 10; ++j)
          __builtin_amdgcn_raw_buffer_store_b128(__builtin_bit_cast(u32x4, rf[j]), rs, rvo + (j % 5) * 1024 + (j / 5) * rstep5, 0, 16 | NT5);
        rn = 0;
        WAITV(0);
        arrive_n(1, WS_F1, 5);
      }
    }
    if (wid == 7 && c1 < 15) {
      if (EV & 1) late_load(7);
      if (EV & 4) late_load(4);
      __builtin_amdgcn_s_sleep(FDLY);
      wait_flags(ws + WS_F0 + (lane < 16 ? g * 16 + lane : (l15 * 16 + c1)) * 128, epoch);
      const u32x4 gs = LD128(lane * 16, WS_ST1 + (unsigned)g * 1024);
      f32x4 t = {};
#pragma unroll
      for (int i = 0; i < 8; ++i) t += __builtin_bit_cast(f32x4, LD128(hi * 4096 + l31 * 16, WS_CS1 + (unsigned)(c1 * 16 + i) * 512));
#pragma unroll
      for (int j = 0; j < 4; ++j) t[j] = sum32(t[j]);
      *(LAS u32x4*)(sm + L_STAT + lane * 16) = gs;
      if (lane < 32) *(LAS f32x4*)(sm + L_CS + lane * 16) = t;
      *(volatile LAS u32x2*)(sm + L_W + W_EPO) = u32x2{(unsigned)epoch, (unsigned)(epoch >> 32)};
      wset(W_STATS, 1);
      STAMP(19);
    }
    if (wid >= 4) {
      if (!((EV & 1) && wid == 7 && c1 < 15)) late_load(wid);
      if ((EV & 2) && wid == 6 && c1 < 15) { late_load(5); WAITV(0); wset(W_LL, 1); }
      late_read();
    }
    STAMP(17);
    if (wid == 6) {
      if (c1 < 15) { for (int sp = 0; wget(W_CNT + 4) < 4 && sp < SPIN_CAP; ++sp) __builtin_amdgcn_s_sleep(1); __builtin_amdgcn_s_sleep(GDLY); }
      else wait_flags(ws + WS_HINT + g * 128, epoch);
      wwait(W_S1DONE, 4);
      const int i = lane < 18 ? lane : 0;
      const int which = i / 6, po = which ? (i % 6) >> 1 : 0, e = i & 1;
      const int tile = (640 * which + 40 * c2 + 39 * e) >> 7;
      const int pn = g2 - po < 0 ? 0 : g2 - po;
      wait_flags(ws + WS_F1 + (pn * 16 + tile) * 128, epoch);
      wset(W_GO, 1);
    }
  }
  const char* wsrc3 = ws + WS_W3 + (c & 7) * 102400 + (wid & 1) * 10240;
  const char* wsrc4 = ws + WS_W4 + c * 245760 + (wid & 1) * 10240 - 5 * 20480;
  const char* wsrc5 = ws + WS_W5 + (c & 7) * 245760 + (wid & 1) * 10240 - 17 * 20480;
  auto wissue_half = [&](int st, int h) {
    const char* sb = (st < 5 ? wsrc3 : st < 17 ? wsrc4 : wsrc5) + st * 20480 + h * 5120;
    glds5_sc1(lane * 16, sb, sm0 + L_RING + ((st + 3) % 7) * RSLOT + (wid & 1) * 10240 + h * 5120);
  };
  STAMP(2);

  bool wready;
  {
    const int hs = wid >> 2, qb = wid & 3;
    if (!isC) {
      const int bk = wid & 3, pn = (bk == 3 || g2 - bk < 0) ? g2 : g2 - bk;
      const char* sA = ws + WS_QKV + (size_t)(pn * 240 + (bk == 3 ? 0 : 80) + 5 * c2) * 2048;
      const unsigned dA = sm0 + (bk == 3 ? L_OST : L_K + (2 - bk) * 2048), dstr = bk == 3 ? 2048 : 6144, dB = sm0 + L_V + (2 - bk) * 2048;
      wwait(W_GO, 1);
      STAMP(3);
#pragma unroll
      for (int j = 0; j < 5; ++j) glds2_sc1(lane * 16, sA + j * 2048, dA + j * dstr);
      if (bk < 3) {
#pragma unroll
        for (int j = 0; j < 5; ++j) glds2_sc1(lane * 16, sA + (80 + j) * 2048, dB + j * VSTR);
      }
      if (wid == 7) {
        if (__all(ld_flag(ws + WS_F1 + (l15 * 16 + 15) * 128) == epoch)) wset(W_WREADY, 1);
      }
    }
    const unsigned ak0 = L_K + (3 * hs + hi) * 6144 + l31 * 16, ak1 = L_K + 2 * 6144 + l31 * 16;
    const int kb_lo = (g2 >= 2) ? qb : (g2 == 1 ? 4 : 8), kb_hi = qb + 8;
    const float NINF = -__builtin_inff();
    f32x16 cD, cF;
#pragma unroll
    for (int r = 0; r < 16; ++r) {
      const bool low = (r & 3) + 8 * (r >> 2) + 4 * hi <= l31;
      cD[r] = low ? 0.f : NINF;
      cF[r] = low ? NINF : 0.f;
    }
    const bool isOne = (G & 1) && p4;
    const unsigned av0 = isOne ? L_ONES
                               : L_V + (4 * hi + q4) * 16 + ((G & 1) ? 2 * VSTR + 8 * hs : (3 * hs + (p4 & 1)) * VSTR + 8 * (p4 >> 1));
    const unsigned avs = isOne ? 0 : 512;
    unsigned a0 = ak0 + qb * 512, a1 = hi ? L_KT + qb * 16 : ak1 + qb * 512, av = av0 + qb * avs;
    const unsigned a1s = hi ? 16 : 512;
    asm volatile("" : "+v"(cD), "+v"(cF), "+v"(a0), "+v"(a1), "+v"(av));
    if (wid >= 4 && wid < 7) WAITV(10); else WAITV(0);
    BAR();
    STAMP(4);
    wready = isL && wget(W_WREADY);
    const int row = 32 * qb + l31;
    u32x4 q0 = *(LAS u32x4*)(sm + L_OST + (3 * hs + hi) * 2048 + row * 16), q1 = *(LAS u32x4*)(sm + L_OST + 2 * 2048 + row * 16);
    if (hi || hs) { q1[0] = 0; q1[1] = 0; }
    if (hi || !hs) { q1[2] = 0; q1[3] = 0; }
    const bf16x8 qf0 = __builtin_bit_cast(bf16x8, q0), qf1 = __builtin_bit_cast(bf16x8, q1);
    auto vmax = [&](const f32x16& s, float m) {
#pragma unroll
      for (int r = 0; r < 16; r += 2) m = fmaxf(fmaxf(m, s[r]), s[r + 1]);
      return m;
    };
    f32x16 o;
    float rl, m;
    auto finish = [&]() {
      auto sw = __builtin_amdgcn_permlane32_swap(__float_as_uint(o[8]), __float_as_uint(o[8]), false, false);
      const float l = __uint_as_float(sw[1]);
      rl = __builtin_amdgcn_rcpf(l);
      float am = 0.f;
#pragma unroll
      for (int r = 0; r < 12; r += 2) am = fmaxf(fmaxf(am, fabsf(o[r])), fabsf(o[r + 1]));
      return (bool)__all(l <= 0x1p30f && l >= 0x1p-30f && am < __builtin_inff());
    };
    {
      f32x16 s[2];
      bf16x8 k0 = ld_b128(sm + a0 + 8 * 512), k1 = ld_b128(sm + a1 + 8 * a1s);
      s[1] = mfma32_c(k0, qf0, cD);
      s[0] = mfma32_c(k1, qf1, s[1]);
      asm volatile("s_nop 11" : "+v"(s[0]));
      m = max32(vmax(s[0], NINF));
      u32x4 q2 = q1;
      if (hi) q2[0] = pk2(-m, -3.0e38f);
      const bf16x8 qf2 = __builtin_bit_cast(bf16x8, q2);
      s[0] = MFMA32(k1, qf2, s[1]);
      k0 = ld_b128(sm + a0); k1 = ld_b128(sm + a1);
      o = f32x16{};
      SCHED();
      WAITV(0);
      BAR();
      SCHED();
#pragma unroll
      for (int j = 0; j < 9; ++j) {
        if (APRIO == 1) { if (((j + hs) & 1)) __builtin_amdgcn_s_setprio(1); else __builtin_amdgcn_s_setprio(0); }
        if (APRIO == 2 && j == 0 && hs) __builtin_amdgcn_s_setprio(1);
        if (APRIO == 2 && j == 5 && hs) __builtin_amdgcn_s_setprio(0);
        f32x16& sc = s[j & 1];
        f32x16& sn = s[(j + 1) & 1];
        unsigned avj = av + (j == 0 ? 8 : j - 1) * avs;
        bf16x8 vf0 = ld_tr2(sm + avj, sm + avj + 128), vf1 = ld_tr2(sm + avj + 256, sm + avj + 384);
        unsigned pw[8];
        if (j < 8) sn = MFMA32(k0, qf0, j == 0 ? cF : f32x16{});
        SCHED();
#pragma unroll
        for (int r = 0; r < 2; ++r) pw[r] = pk2(__builtin_amdgcn_exp2f(sc[2 * r]), __builtin_amdgcn_exp2f(sc[2 * r + 1]));
        SCHED();
        if (j < 8) sn = MFMA32(k1, qf2, sn);
        if (j < 7) { a1 += a1s; k0 = ld_b128(sm + a0 + (j + 1) * 512); k1 = ld_b128(sm + a1); }
        SCHED();
#pragma unroll
        for (int r = 2; r < 5; ++r) pw[r] = pk2(__builtin_amdgcn_exp2f(sc[2 * r]), __builtin_amdgcn_exp2f(sc[2 * r + 1]));
        SCHED();
        o = MFMA32(vf0, __builtin_bit_cast(bf16x8, (u32x4{pw[0], pw[1], pw[2], pw[3]})), o);
        SCHED();
#pragma unroll
        for (int r = 5; r < 8; ++r) pw[r] = pk2(__builtin_amdgcn_exp2f(sc[2 * r]), __builtin_amdgcn_exp2f(sc[2 * r + 1]));
        SCHED();
        o = MFMA32(vf1, __builtin_bit_cast(bf16x8, (u32x4{pw[4], pw[5], pw[6], pw[7]})), o);
        SCHED();
        if (j < 5 && 2 * j < rn) { retile_store(2 * j); retile_store(2 * j + 1); }
        if (j < 8 && wready) wissue_half(j >> 1, j & 1);
      }
    }
    if (APRIO) __builtin_amdgcn_s_setprio(0);
    if (!finish()) {
      int lq = l31 - 4 * hi;
      OPAQUE(lq);
#pragma unroll 1
      for (int k2 = kb_lo; k2 < kb_hi; ++k2) {
        f32x16 cz;
#pragma unroll
        for (int r = 0; r < 16; ++r) cz[r] = (k2 == qb && (r & 3) + 8 * (r >> 2) <= lq) ? NINF : 0.f;
        f32x16 s = MFMA32(ld_b128(sm + ak0 + k2 * 512), qf0, cz);
        s = MFMA32(ld_b128(sm + ak1 + k2 * 512), qf1, s);
        m = vmax(s, m);
      }
      m = max32(m);
      o = f32x16{};
#pragma unroll 1
      for (int kb = kb_lo; kb <= kb_hi; ++kb) {
        f32x16 cin;
#pragma unroll
        for (int r = 0; r < 16; ++r) cin[r] = ((r & 3) + 8 * (r >> 2) <= lq ? kb == qb : kb == kb_hi) ? NINF : -m;
        f32x16 s = MFMA32(ld_b128(sm + ak0 + kb * 512), qf0, cin);
        s = MFMA32(ld_b128(sm + ak1 + kb * 512), qf1, s);
        const unsigned av = av0 + kb * avs;
        bf16x8 vf0 = ld_tr2(sm + av, sm + av + 128), vf1 = ld_tr2(sm + av + 256, sm + av + 384);
        unsigned pw[8];
#pragma unroll
        for (int r = 0; r < 8; ++r) pw[r] = pk2(__builtin_amdgcn_exp2f(s[2 * r]), __builtin_amdgcn_exp2f(s[2 * r + 1]));
        o = MFMA32(vf0, __builtin_bit_cast(bf16x8, (u32x4{pw[0], pw[1], pw[2], pw[3]})), o);
        o = MFMA32(vf1, __builtin_bit_cast(bf16x8, (u32x4{pw[4], pw[5], pw[6], pw[7]})), o);
      }
      (void)finish();
    }
    const unsigned ob = WS_O + (unsigned)(g2 * 80 + 5 * c2) * 2048;
    ST128((u32x4{pk2(o[0] * rl, o[1] * rl), pk2(o[2] * rl, o[3] * rl), pk2(o[4] * rl, o[5] * rl), pk2(o[6] * rl, o[7] * rl)}),
          (3 * hs + hi) * 2048 + row * 16, ob);
    if (!hi) ST64((u32x2{pk2(o[8] * rl, o[9] * rl), pk2(o[10] * rl, o[11] * rl)}), row * 16 + 8 * hs, ob + 2 * 2048);
    STAMP(5);
    FENCE();
    if (lane == 0) __hip_atomic_fetch_add((LAS unsigned*)(sm + L_W + W_CNT + 8), 1u, __ATOMIC_RELAXED, __HIP_MEMORY_SCOPE_WORKGROUP);
    WAITV(0);
    if (lane == 0) {
      const unsigned old = __hip_atomic_fetch_add((LAS unsigned*)(sm + L_W + W_CNT + 12), 1u, __ATOMIC_RELAXED, __HIP_MEMORY_SCOPE_WORKGROUP);
      if (old == 7) { st_flag(ws + WS_F2 + (g2 * 16 + c2) * 128, epoch); st_flag(ws + WS_F1P + wgid * 8, epoch); }
    }
    STAMP(6);
    if (isC) wwait(W_CNT + 12, 8);
  }

  if (isL) {
    int issued = 4;
    if (!wready) {
#pragma unroll 1
      for (int i = 0; i < 1; ++i) wait_flags(ws + WS_F1 + (l15 * 16 + 15) * 128, epoch);
      issued = 0;
    }
    const int wl = W_LANDW + 2 * (wid & 1);
    if (wready) wset16(wl, 4);
    wwait(W_CNT + 8, 8);
    for (; issued < 5; ++issued) { const int st = rfl(issued); wissue_half(st, 0); wissue_half(st, 1); }
    WAITV(0);
    wset16(wl, 5);
    __builtin_amdgcn_s_sleep(LDLY);
    {
      u64 f[4];
#pragma unroll
      for (int i = 0; i < 4; ++i) f[i] = ld_flag(ws + WS_F1P + i * 512 + lane * 8);
#pragma unroll
      for (int i = 0; i < 4; ++i) wait_flags_from(ws + WS_F1P + i * 512 + lane * 8, f[i], epoch);
    }
    STAMP(30);
    if (wid == 4) wset(W_GO, 2);
    const char* p = wsrc4 + 5 * 20480;
    unsigned dst = sm0 + L_RING + RSLOT + (wid & 1) * 10240;
#pragma unroll 1
    for (int st = 5; st < 29; ++st) {
      if (st == 7 + P3REL) { WAITV(0); wset16(wl, st); }
      for (int sp = 0; wget4(W_CONSW) < (st < 7 ? 1 : st - 5) && sp < SPIN_CAP; ++sp) __builtin_amdgcn_s_sleep(1);
      FENCE();
      glds10_sc1(lane * 16, p, dst);
      p = st == 16 ? wsrc5 + 17 * 20480 : p + 20480;
      dst = dst + RSLOT >= sm0 + L_RING + 7 * RSLOT ? dst - 6 * RSLOT : dst + RSLOT;
      WAITV(20);
      if (st >= 7 && (st < 7 + P3REL || st > 8 + P3REL)) wset16(wl, st - 1);
      STAMP(31 + st - 1);
    }
    WAITV(10);
    wset16(wl, 28);
    WAITV(0);
    wset16(wl, 29);
    STAMP(60);
    STAMP_DUMP();
    return;
  }
  if (!isC) {
    if (wid == 6) {
#pragma unroll 1
      for (int k = 0; k < 2; ++k) {
        for (int sp = 0; (k < 1 ? wget4(W_DONE) <= k : wget(W_CNT + 20) < 4) && sp < SPIN_CAP; ++sp) __builtin_amdgcn_s_sleep(2);
        __builtin_amdgcn_s_sleep(PDLY);
        wait_flags(ws + WS_F4 + k * FL + (g * 16 + l15) * 128, epoch);
        wset(W_RDY, k + 3);
        STAMP(22 + k);
      }
    } else {
      for (int sp = 0; wget(W_GO) < 2 && sp < SPIN_CAP; ++sp) __builtin_amdgcn_s_sleep(4);
      FENCE();
      if (lane < 48) {
        f32x4 t = {};
#pragma unroll
        for (int i = 0; i < 27; ++i) t += __builtin_bit_cast(f32x4, LD128(lane * 16, WS_CSUM + (unsigned)(i * 12288 + c * 768)));
#pragma unroll
        for (int e = 0; e < 4; ++e) {
          const unsigned ch = pk2(t[e], t[e]);
          *(LAS u32x4*)(sm + L_WX + lane * 64 + e * 16) = u32x4{ch, pk2(t[e] - bflo(ch), 0.f), 0, 0};
        }
      }
      wset(W_WREADY, 2);
#pragma unroll 1
      for (int k = 0; k < 1; ++k) {
        for (int sp = 0; wget4(W_DONE) <= k && sp < SPIN_CAP; ++sp) __builtin_amdgcn_s_sleep(1);
        FENCE();
        if (lane == 0) st_flag(ws + WS_F4 + k * FL + wgid * 128, epoch);
        STAMP(22 + k);
      }
      if (b == 0) {
#pragma unroll 1
        for (int i = 0; i < 4; ++i) wait_flags(ws + WS_F2 + (i * 64 + lane) * 128, epoch);
        if (lane == 0) st_flag(ws + WS_EPOCH, epoch);
      }
    }
    STAMP_DUMP();
    return;
  }

  const int wdn = W_DONE + 4 * wid;
  const int wc = W_CONSW + 4 * wid;
  const unsigned lane16 = lane * 16;

  const int r3 = 64 * (c >> 3) + 16 * wid + l15;
  {
    unsigned hpk[10];
    const char* xp = x + (size_t)(128 * g + r3) * 1280 + (c & 7) * 160;
    const u32x4 xr0 = *(const u32x4*)(xp + G * 16), xr1 = *(const u32x4*)(xp + 64 + G * 16);
    const u32x2 xr2 = *(const u32x2*)(xp + 128 + G * 8);
    u32x4 xf[20];

    f32x4 acc[5] = {};
    {
      constexpr int NB = 15;
      bf16x8 wf[NB];
      unsigned so = 0, lv = 0;
      auto rd = [&](int f) {
        if (f == 80) wwait2(W_LANDW, lv, 5);
        if (f % 20 == 0) { so = lane16; OPAQUE(so); so += slot_of(f / 20); OPAQUE(so); }
        wf[f % NB] = ld_b128(sm + so + (f % 20) * 1024);
        if (f == 64) lv = wraw(W_LANDW);
      };
      __builtin_amdgcn_s_sleep(PDLY - 4);
      wwait2(W_LANDW, 0u, 4);
#pragma unroll
      for (int f = 0; f < NB; ++f) rd(f);
      SCHED();
      wait_flags(ws + WS_F2 + (g * 16 + l15) * 128, epoch);
      STAMP(7);
#pragma unroll
      for (int s = 0; s < XW3; ++s) xf[s] = LD128(G * 2048 + r3 * 16, WS_O + (unsigned)(g * 80 + 4 * s) * 2048);
      SCHED();
#pragma unroll
      for (int f = 0; f < 100; ++f) {
        acc[f % 5] = MFMA16(wf[f % NB], __builtin_bit_cast(bf16x8, xf[f / 5]), acc[f % 5]);
        SCHED();
        if (f + NB < 100) rd(f + NB);
        if (f % 3 == 0 && XW3 + f / 3 < 20) xf[XW3 + f / 3] = LD128(G * 2048 + r3 * 16, WS_O + (unsigned)(g * 80 + 4 * (XW3 + f / 3)) * 2048);
        static_assert(3 * (20 - XW3) + NB >= 20 * P3REL - 1, "released stages are read");
        if (f == 3 * (20 - XW3)) wset(wc, 1 + P3REL);
        SCHED();
      }
    }
    STAMP(8);
    float s1 = 0.f, s2 = 0.f;
#pragma unroll
    for (int d = 0; d < 10; ++d) {
      const unsigned xv = d < 4 ? xr0[d & 3] : d < 8 ? xr1[d & 3] : xr2[d & 1];
      const unsigned a = pk2(acc[d >> 1][2 * (d & 1)], acc[d >> 1][2 * (d & 1) + 1]);
      const unsigned h = pk2(bflo(a) + bflo(xv), bfhi(a) + bfhi(xv));
      hpk[d] = h;
      s1 += bflo(h) + bfhi(h);
      s2 += bflo(h) * bflo(h) + bfhi(h) * bfhi(h);
    }
    const unsigned hb = WS_H + (unsigned)(g * 80 + 10 * (c & 7)) * 2048;
    ST128((u32x4{hpk[0], hpk[1], hpk[2], hpk[3]}), G * 2048 + r3 * 16, hb);
    ST128((u32x4{hpk[4], hpk[5], hpk[6], hpk[7]}), G * 2048 + r3 * 16, hb + 4 * 2048);
    {
      auto a0 = __builtin_amdgcn_permlane16_swap(hpk[8], hpk[8], false, false);
      auto a1 = __builtin_amdgcn_permlane16_swap(hpk[9], hpk[9], false, false);
      if (!(G & 1)) ST128((u32x4{a0[0], a1[0], a0[1], a1[1]}), (G >> 1) * 2048 + r3 * 16, hb + 8 * 2048);
    }
    s1 = sum32(sum16(s1)); s2 = sum32(sum16(s2));
    if (G == 0) ST64((u32x2{__float_as_uint(s1), __float_as_uint(s2)}), r3 * 8, WS_STAT + (unsigned)(g * 8 + (c & 7)) * 1024);
    *(LAS u32x4*)(sm + L_OST + wid * 2560 + lane * 16) = u32x4{hpk[0], hpk[1], hpk[2], hpk[3]};
    *(LAS u32x4*)(sm + L_OST + wid * 2560 + 1024 + lane * 16) = u32x4{hpk[4], hpk[5], hpk[6], hpk[7]};
    *(LAS u32x2*)(sm + L_OST + wid * 2560 + 2048 + lane * 8) = u32x2{hpk[8], hpk[9]};
    WAITV(0);
    arrive_w(4, WS_F3);
    STAMP(9);
  }

  {
    constexpr int NB = 6;
    bf16x8 wf[NB];
    unsigned so = 0;
    unsigned lv = 0;
    auto rd = [&](int n) __attribute__((always_inline)) {
      const int part = n / 123, i = n % 123, f = 120 * part + i;
      if (i >= 120) {
        if (n == 120) lds_wait_ge(sm0 + L_W + W_WREADY, 2);
        wf[n % NB] = ld_b128(sm + L_WX + (3 * part + i - 120) * 512 + (lane16 & 511));
      } else {
      if (f % 20 == 0) { wwait2(W_LANDW, lv, 5 + f / 20 + 1); so = lane16; OPAQUE(so); so += slot_of(5 + f / 20); OPAQUE(so); }
      wf[n % NB] = ld_b128(sm + so + (f % 20) * 1024);
      if (f % 20 == 12) lv = wraw(W_LANDW);
      if (f % 20 == 19) wset(wc, 5 + f / 20 + 2);
      }
    };
    __builtin_amdgcn_s_sleep(PDLY - 2);
#pragma unroll
    for (int n = 0; n < NB; ++n) rd(n);
    SCHED();
    wait_flags(ws + WS_F3 + (g * 16 + l15) * 128, epoch);
    STAMP(10);
    const int r4 = 32 * wid + l31;
    const unsigned vx = hi * 2048 + r4 * 16;
    const unsigned hb = WS_H + (unsigned)(g * 80) * 2048;
    u32x4 xf[40];
    u32x2 stp[4];
    constexpr int XW = 10;
#pragma unroll
    for (int i = 0; i < XW; ++i) xf[i] = LD128(vx, hb + i * 4096);
    SCHED();
    float r2 = 0.f;
    unsigned xe0 = 0, xe1 = 0;
    static_for<0, 2>([&](auto pc) __attribute__((always_inline)) {
      constexpr int part = pc.value;
      f32x16 acc[3] = {};
      static_for<0, 123>([&](auto ic) __attribute__((always_inline)) {
        constexpr int i = ic.value;
        const int n = 123 * part + i;
        if (i % 20 == 0 && n > 0 && i < 120) { STAMP2(42 + (120 * part + i) / 20); SCHED(); }
        acc[i % 3] = MFMA32(wf[n % NB], __builtin_bit_cast(bf16x8, i < 120 ? xf[i < 120 ? i / 3 : 0] : u32x4{xe0, xe1, 0, 0}), acc[i % 3]);
        SCHED();
        if (n + NB < 246) rd(n + NB);
        if (part == 0 && (i & 1) && (i >> 1) + XW < 40) xf[(i >> 1) + XW] = LD128(vx, hb + ((i >> 1) + XW) * 4096);
        if (n >= 4 && n < 12 && !(n & 1)) stp[(n - 4) >> 1] = LD64(hi * 4096 + r4 * 8, WS_STAT + (unsigned)(g * 8 + ((n - 4) >> 1)) * 1024);
        if (n == 44) {
          float s1 = 0.f, s2 = 0.f;
#pragma unroll
          for (int j = 0; j < 4; ++j) { s1 += __uint_as_float(stp[j][0]); s2 += __uint_as_float(stp[j][1]); }
          s1 = sum32(s1) * (1.f / 640.f); s2 = sum32(s2) * (1.f / 640.f);
          r2 = __builtin_amdgcn_rcpf(fmaxf(s2 - s1 * s1, 0.f) + 1e-5f);
          const unsigned mh = pk2(-s1, 0.f);
          xe0 = hi ? 0u : pk2(-s1, -s1 - bflo(mh));
          xe1 = hi ? 0u : mh;
          OPAQUE(r2); OPAQUE(xe0); OPAQUE(xe1);
        }
        SCHED();
        if (part == 1 && i == 36) { WAITV(0); wset(wdn, 1); SCHED(); }
      });
      STAMP2(54 + part);
      if (part == 1) STAMP(11);
#pragma unroll
      for (int t = 0; t < 3; ++t) {
        unsigned o[4];
#pragma unroll
        for (int d = 0; d < 4; ++d) {
          const f32x2 hv = (f32x2{acc[t][2 * d], acc[t][2 * d + 1]} * r2) * f32x2{acc[t][8 + 2 * d], acc[t][8 + 2 * d + 1]};
          o[d] = pk2(hv[0], hv[1]);
        }
        ST128((u32x4{o[0], o[1], o[2], o[3]}), vx, WS_HH + (unsigned)(g * 192 + 96 * part + 6 * c + 2 * t) * 2048);
      }
      SCHED();
    });
    STAMP(12);
  }

  {
    unsigned ln5 = lane16;
    OPAQUE(ln5);
    const int wk = wid & 1;
    const unsigned hb5 = WS_HH + (unsigned)(g * 192) * 2048 + wk * 16384;
    const unsigned v50 = (ln5 >> 8) * 2048 + (64 * (c >> 3) + 16 * wid) * 16 + (ln5 & 255), v51 = v50 ^ 256;
    f32x4 acc[5] = {}, acp[5] = {};
    constexpr int NB = 8;
    bf16x8 wf[NB];
    u32x4 x5[24];
    unsigned so = 0;
    unsigned lv = 0;
    auto rd = [&](int e) {
      if (e % 10 == 0) { wwait2(W_LANDW, lv, 17 + e / 10 + 1); so = ln5 + wk * 10240; OPAQUE(so); so += slot_of(17 + e / 10); OPAQUE(so); }
      wf[e % NB] = ld_b128(sm + so + (e % 10) * 1024);
      if (e % 10 == 3) lv = wraw(W_LANDW);
      if (e % 10 == 9) wset(wc, 17 + e / 10 + 2);
    };
    auto ldx = [&](int part, int j) {
      const int kk = 12 * part + (j >> 1);
      x5[j] = LD128((j & 1) ? v51 : v50, hb5 + (4 * (kk >> 1) + (kk & 1)) * 8192);
    };
    lds_wait_ge(sm0 + L_W + W_RDY, 3);
    STAMP(40);
#pragma unroll
    for (int j = 0; j < XW5; ++j) ldx(0, j);
#pragma unroll
    for (int e = 0; e < NB; ++e) rd(e);
    SCHED();
    WAITV(XW5);
    arrive_w(5, WS_F4 + FL);
    STAMP(13);
    SCHED();
#pragma unroll
    for (int part = 0; part < 2; ++part) {
#pragma unroll
      for (int i = 0; i < 60; ++i) {
        const int e = 60 * part + i;
        acc[i % 5] = MFMA16(wf[e % NB], __builtin_bit_cast(bf16x8, x5[2 * (i / 5)]), acc[i % 5]);
        SCHED();
        acp[i % 5] = MFMA16(wf[e % NB], __builtin_bit_cast(bf16x8, x5[2 * (i / 5) + 1]), acp[i % 5]);
        SCHED();
        if (e + NB < 120) rd(e + NB);
        if (XW5 + i < 24) ldx(part, XW5 + i);
        SCHED();
      }
      if (part == 0) {
        lds_wait_ge(sm0 + L_W + W_RDY, 4);
        STAMP(41);
#pragma unroll
        for (int j = 0; j < XW5; ++j) ldx(1, j);
        SCHED();
      }
    }
    for (int sp = 0; wget4(W_CONSW) < 24 && sp < SPIN_CAP; ++sp) {}
    FENCE();
#pragma unroll
    for (int tt = 0; tt < 5; ++tt) *(LAS f32x4*)(sm + slot_of(22) + (wid ^ 1) * 5120 + tt * 1024 + ln5) = acp[tt];
    wset(wdn, 9);
    lds_wait_ge(sm0 + L_W + W_DONE + 4 * (wid ^ 1), 9);
#pragma unroll
    for (int tt = 0; tt < 5; ++tt) acc[tt] += *(LAS f32x4*)(sm + slot_of(22) + wid * 5120 + tt * 1024 + ln5);
    STAMP(14);
    unsigned opk[10];
    const u32x4 h0 = *(LAS u32x4*)(sm + L_OST + wid * 2560 + ln5), h1 = *(LAS u32x4*)(sm + L_OST + wid * 2560 + 1024 + ln5);
    const u32x2 h2 = *(LAS u32x2*)(sm + L_OST + wid * 2560 + 2048 + (ln5 >> 1));
#pragma unroll
    for (int d = 0; d < 10; ++d) {
      const unsigned hv = d < 4 ? h0[d & 3] : d < 8 ? h1[d & 3] : h2[d & 1];
      const unsigned a = pk2(acc[d >> 1][2 * (d & 1)], acc[d >> 1][2 * (d & 1) + 1]);
      opk[d] = pk2(bflo(a) + bflo(hv), bfhi(a) + bfhi(hv));
    }
    const unsigned G5 = ln5 >> 8;
    char* op = out + (size_t)(128 * g + 64 * (c >> 3) + 16 * wid) * 1280 + (c & 7) * 160 + (ln5 & 255) * 80;
    *(u32x4*)(op + G5 * 16) = u32x4{opk[0], opk[1], opk[2], opk[3]};
    *(u32x4*)(op + 64 + G5 * 16) = u32x4{opk[4], opk[5], opk[6], opk[7]};
    *(u32x2*)(op + 128 + G5 * 8) = u32x2{opk[8], opk[9]};
    STAMP(15);
    STAMP_DUMP();
  }
}

extern "C" void kernel_launch(void* const* d_in, const int* in_sizes, int n_in, void* d_out, int out_size, void* d_ws, size_t ws_size,
                              hipStream_t stream) {
  fused_block<<<256, 512, 0, stream>>>((const char*)d_in[0], (const char*)d_in[1], (const char*)d_in[2], (const char*)d_in[4],
                                       (const char*)d_in[5], (const char*)d_in[6], (char*)d_out, (char*)d_ws);
}
```

```cpp
#include <hip/hip_runtime.h>
#include <stdint.h>

typedef unsigned long long u64;
typedef __attribute__((ext_vector_type(8))) short bf16x8;
typedef __attribute__((ext_vector_type(4))) short s16x4;
typedef __attribute__((ext_vector_type(2))) __bf16 bf16x2;
typedef __attribute__((ext_vector_type(16))) float f32x16;
typedef __attribute__((ext_vector_type(4))) float f32x4;
typedef __attribute__((ext_vector_type(2))) float f32x2;
typedef __attribute__((ext_vector_type(4))) unsigned u32x4;
typedef __attribute__((ext_vector_type(2))) unsigned u32x2;
#define LAS __attribute__((address_space(3)))
#define DI __device__ __forceinline__

constexpr unsigned WS_EPOCH = 0, WS_HINT = 1024;
constexpr unsigned FL = 256 * 128;
constexpr unsigned WS_F0 = 4096, WS_F1 = WS_F0 + FL, WS_F2 = WS_F1 + FL, WS_F3 = WS_F2 + FL, WS_F4 = WS_F3 + FL;
constexpr unsigned WS_F1P = WS_F4 + 3 * FL;
constexpr unsigned WS_CSUM = WS_F1P + 2048;
constexpr unsigned WS_ST1 = WS_CSUM + 27 * 12288;
constexpr unsigned WS_CS1 = WS_ST1 + 2048 * 16;
constexpr unsigned WS_STAT = WS_CS1 + 16 * 16 * 512;
constexpr unsigned WS_QKV = 1u << 20;
constexpr unsigned WS_O = WS_QKV + 16 * 240 * 2048;
constexpr unsigned WS_H = WS_O + 16 * 80 * 2048;
constexpr unsigned WS_HH = WS_H + 16 * 80 * 2048;
constexpr unsigned WS_W3 = WS_HH + 16 * 192 * 2048;
constexpr unsigned WS_W4 = WS_W3 + 8 * 100 * 1024;
constexpr unsigned WS_W5 = WS_W4 + 16 * 240 * 1024;
constexpr unsigned WS_END = WS_W5 + 8 * 240 * 1024;
constexpr unsigned WS_DIAG = 32u << 20;
static_assert(WS_STAT + 16 * 8 * 1024 <= WS_QKV && WS_END <= WS_DIAG, "workspace map");

constexpr unsigned L_STAT = 0, L_CS = 1024;
constexpr unsigned L_W = 1536;
constexpr unsigned L_ONES = 1792;
constexpr unsigned L_KT = 2304;
constexpr unsigned L_RP = 2560;
constexpr unsigned L_OST = 2560;
constexpr unsigned L_JUNK = 31488;
constexpr unsigned L_HSTG = 81920;
constexpr unsigned L_WX = 12800;
constexpr int CODE_LINES = 48;
constexpr unsigned L_P1 = 32768;
constexpr unsigned L_K = 12800, L_V = L_K + 5 * 6144, VSTR = 6208;
constexpr unsigned L_RING = 20480, RSLOT = 20480;
constexpr unsigned slot_of(int i) { return L_RING + ((i + 3) % 7) * RSLOT; }
static_assert(L_V + 5 * VSTR <= L_RING + 3 * RSLOT, "LDS map");
enum { W_LAND1 = 0, W_LANDW = 8, W_CONS1 = 16, W_CONSW = 32, W_GO = 48, W_S1DONE = 52, W_STATS = 56, W_WREADY = 60, W_RDY = 64, W_DONE = 80, W_CNT = 96, W_INIT = 128, W_EPO = 176, W_LL = 184 };

#define P4REL 0
#define APRIO 2
#define PDLY 24
#define PSLP 1
#define GDLY 16
#define FDLY 1
#define XW3 8
#define LDLY 16
#define FPUB 36
#define FX2 128
#define ABL 0
#define AMAP 1
#define XW5 10
#define P3REL 3
#define EV 7
#define LP 0
#define NTS " nt"
#define NT1 ""
#define NT2 0
#define NT3 0
#define NT5 0
#define NT6 0
#define NT4 0
#define XW5B 10
#define XW5I 10
#define XW4 10
#define RS 0
#define R1C 0
#define R1G 0
#define LP1 0
#define LP2 0
#define LA 2
#define PB 0
#define LSL 0
#define P3LATE 1
#define TCS 0
#define P4H 1
#define WJ 8
#define RE 0
#define FLW 16
#define X3S 3
#define WIW 1
#define AJ 6
constexpr float QSCALE = 0.22360679774997896f * 1.4426950408889634f;

#define MFMA32(a, b, c) __builtin_amdgcn_mfma_f32_32x32x16_bf16((a), (b), (c), 0, 0, 0)
#define MFMA16(a, b, c) __builtin_amdgcn_mfma_f32_16x16x32_bf16((a), (b), (c), 0, 0, 0)
#define BAR() asm volatile("s_waitcnt lgkmcnt(0)\n\ts_barrier" ::: "memory")
#define SCHED() __builtin_amdgcn_sched_barrier(0)
#define RAWBAR() do { SCHED(); asm volatile("s_barrier" ::: "memory"); SCHED(); } while (0)
#define OPAQUE(v) asm volatile("" : "+v"(v))
#define SGB(m, n) __builtin_amdgcn_sched_group_barrier((m), (n), 0)
#define WAITV(n) asm volatile("s_waitcnt vmcnt(%0)" ::"i"(n) : "memory")

#define STAMP(i) do {} while (0)
#define STAMP2(i) do {} while (0)
#define STAMP_DUMP() do {} while (0)

DI void glds(unsigned voff, const char* sbase, unsigned ldst) {
  unsigned sv;
  asm volatile("s_mov_b32 %0, m0\n\ts_mov_b32 m0, %3\n\ts_nop 2\n\tglobal_load_lds_dwordx4 %1, %2" NTS "\n\ts_mov_b32 m0, %0"
               : "=&s"(sv) : "v"(voff), "s"(sbase), "s"(ldst) : "memory");
}
DI void glds_sc1(unsigned voff, const char* sbase, unsigned ldst) {
  unsigned sv;
  asm volatile("s_mov_b32 %0, m0\n\ts_mov_b32 m0, %3\n\ts_nop 2\n\tglobal_load_lds_dwordx4 %1, %2 sc1\n\ts_mov_b32 m0, %0"
               : "=&s"(sv) : "v"(voff), "s"(sbase), "s"(ldst) : "memory");
}
DI void glds5_sc1(unsigned voff, const char* sbase, unsigned ldst) {
  unsigned sv;
  asm volatile("s_mov_b32 %0, m0\n\ts_mov_b32 m0, %3\n\ts_nop 2\n\t"
               "global_load_lds_dwordx4 %1, %2 offset:-2048 sc1\n\tglobal_load_lds_dwordx4 %1, %2 offset:-1024 sc1\n\t"
               "global_load_lds_dwordx4 %1, %2 sc1\n\tglobal_load_lds_dwordx4 %1, %2 offset:1024 sc1\n\t"
               "global_load_lds_dwordx4 %1, %2 offset:2048 sc1\n\ts_mov_b32 m0, %0"
               : "=&s"(sv) : "v"(voff), "s"(sbase + 2048), "s"(ldst + 2048) : "memory");
}
DI void glds5(unsigned voff, const char* sbase, unsigned ldst) {
  unsigned sv;
  asm volatile("s_mov_b32 %0, m0\n\ts_mov_b32 m0, %3\n\ts_nop 2\n\t"
               "global_load_lds_dwordx4 %1, %2 offset:-2048" NTS "\n\tglobal_load_lds_dwordx4 %1, %2 offset:-1024" NTS "\n\t"
               "global_load_lds_dwordx4 %1, %2" NTS "\n\tglobal_load_lds_dwordx4 %1, %2 offset:1024" NTS "\n\t"
               "global_load_lds_dwordx4 %1, %2 offset:2048" NTS "\n\ts_mov_b32 m0, %0"
               : "=&s"(sv) : "v"(voff), "s"(sbase + 2048), "s"(ldst + 2048) : "memory");
}
DI void glds8(const unsigned (&v)[8], const char* sbase, unsigned ldst) {
  unsigned sv;
  asm volatile("s_mov_b32 %0, m0\n\ts_mov_b32 m0, %10\n\ts_nop 2\n\t"
               "global_load_lds_dwordx4 %1, %9 offset:-4096" NT1 "\n\tglobal_load_lds_dwordx4 %2, %9 offset:-3072" NT1 "\n\t"
               "global_load_lds_dwordx4 %3, %9 offset:-2048" NT1 "\n\tglobal_load_lds_dwordx4 %4, %9 offset:-1024" NT1 "\n\t"
               "global_load_lds_dwordx4 %5, %9" NT1 "\n\tglobal_load_lds_dwordx4 %6, %9 offset:1024" NT1 "\n\t"
               "global_load_lds_dwordx4 %7, %9 offset:2048" NT1 "\n\tglobal_load_lds_dwordx4 %8, %9 offset:3072" NT1 "\n\ts_mov_b32 m0, %0"
               : "=&s"(sv) : "v"(v[0]), "v"(v[1]), "v"(v[2]), "v"(v[3]), "v"(v[4]), "v"(v[5]), "v"(v[6]), "v"(v[7]),
                 "s"(sbase), "s"(ldst + 4096) : "memory");
}
DI void glds10_sc1(unsigned voff, const char* sbase, unsigned ldst) {
  unsigned sv;
  asm volatile("s_mov_b32 %0, m0\n\ts_mov_b32 m0, %4\n\ts_nop 2\n\t"
               "global_load_lds_dwordx4 %1, %3 offset:-4096 sc1\n\tglobal_load_lds_dwordx4 %1, %3 offset:-3072 sc1\n\t"
               "global_load_lds_dwordx4 %1, %3 offset:-2048 sc1\n\tglobal_load_lds_dwordx4 %1, %3 offset:-1024 sc1\n\t"
               "global_load_lds_dwordx4 %1, %3 sc1\n\tglobal_load_lds_dwordx4 %1, %3 offset:1024 sc1\n\t"
               "global_load_lds_dwordx4 %1, %3 offset:2048 sc1\n\tglobal_load_lds_dwordx4 %1, %3 offset:3072 sc1\n\t"
               "s_add_u32 m0, m0, 0x1000\n\ts_nop 0\n\t"
               "global_load_lds_dwordx4 %2, %3 sc1\n\tglobal_load_lds_dwordx4 %2, %3 offset:1024 sc1\n\ts_mov_b32 m0, %0"
               : "=&s"(sv) : "v"(voff), "v"(voff + 4096), "s"(sbase + 4096), "s"(ldst + 4096) : "memory", "scc");
}
DI void glds2_sc1(unsigned voff, const char* sbase, unsigned ldst) {
  unsigned sv;
  asm volatile("s_mov_b32 %0, m0\n\ts_mov_b32 m0, %3\n\ts_nop 2\n\t"
               "global_load_lds_dwordx4 %1, %2 sc1\n\tglobal_load_lds_dwordx4 %1, %2 offset:1024 sc1\n\ts_mov_b32 m0, %0"
               : "=&s"(sv) : "v"(voff), "s"(sbase), "s"(ldst) : "memory");
}
#define ST128(v, voff, soff) __builtin_amdgcn_raw_buffer_store_b128((v), rs, (unsigned)(voff) + (unsigned)(soff), 0, 16 | NT4)
#define ST128R(v, voff, soff) __builtin_amdgcn_raw_buffer_store_b128((v), rs, (unsigned)(voff) + (unsigned)(soff), 0, 16 | NT3)
#define ST64(v, voff, soff) __builtin_amdgcn_raw_buffer_store_b64((v), rs, (unsigned)(voff) + (unsigned)(soff), 0, 16)
#define LD128(voff, soff) __builtin_amdgcn_raw_buffer_load_b128(rs, (unsigned)(voff), (unsigned)(soff), 16)
#define LD64(voff, soff) __builtin_amdgcn_raw_buffer_load_b64(rs, (unsigned)(voff), (unsigned)(soff), 16)
DI bf16x8 ld_b128(LAS char* p) { return *(LAS bf16x8*)p; }
DI s16x4 ld_tr(LAS char* p) { return __builtin_amdgcn_ds_read_tr16_b64_v4i16((LAS s16x4*)p); }
DI bf16x8 ld_tr2(LAS char* p0, LAS char* p1) {
  s16x4 a = ld_tr(p0), b = ld_tr(p1);
  return __builtin_shufflevector(a, b, 0, 1, 2, 3, 4, 5, 6, 7);
}
DI unsigned pk2(float lo, float hi) {
  f32x2 v = {lo, hi};
  return __builtin_bit_cast(unsigned, __builtin_convertvector(v, bf16x2));
}
DI float bflo(unsigned w) { return __uint_as_float(w << 16); }
DI float bfhi(unsigned w) { return __uint_as_float(w & 0xffff0000u); }
DI float sum32(float v) {
  auto r = __builtin_amdgcn_permlane32_swap(__float_as_uint(v), __float_as_uint(v), false, false);
  return __uint_as_float(r[0]) + __uint_as_float(r[1]);
}
DI float sum16(float v) {
  auto r = __builtin_amdgcn_permlane16_swap(__float_as_uint(v), __float_as_uint(v), false, false);
  return __uint_as_float(r[0]) + __uint_as_float(r[1]);
}
DI float sum8(float v) {
  v += __uint_as_float(__builtin_amdgcn_update_dpp(0, __float_as_uint(v), 0xB1, 0xf, 0xf, true));
  v += __uint_as_float(__builtin_amdgcn_update_dpp(0, __float_as_uint(v), 0x4E, 0xf, 0xf, true));
  v += __uint_as_float(__builtin_amdgcn_update_dpp(0, __float_as_uint(v), 0x141, 0xf, 0xf, true));
  return v;
}
DI float max32(float v) {
  auto r = __builtin_amdgcn_permlane32_swap(__float_as_uint(v), __float_as_uint(v), false, false);
  return fmaxf(__uint_as_float(r[0]), __uint_as_float(r[1]));
}
DI u64 mix64(u64 z) {
  z += 0x9E3779B97F4A7C15ull; z = (z ^ (z >> 30)) * 0xBF58476D1CE4E5B9ull; z = (z ^ (z >> 27)) * 0x94D049BB133111EBull;
  return z ^ (z >> 31);
}
DI u64 ld_flag(const char* p) { return __hip_atomic_load((const u64*)p, __ATOMIC_RELAXED, __HIP_MEMORY_SCOPE_AGENT); }
DI void st_flag(char* p, u64 v) { __hip_atomic_store((u64*)p, v, __ATOMIC_RELAXED, __HIP_MEMORY_SCOPE_AGENT); }
DI void wait_flags_from(const char* p, u64 v, u64 epoch) {
  for (unsigned spins = 0; spins < (1u << 18); ++spins) {
    if (__all(v == epoch)) break;
    __builtin_amdgcn_s_sleep(PSLP);
    v = ld_flag(p);
  }
  asm volatile("" ::: "memory");
}
DI void wait_flags(const char* p, u64 epoch) { wait_flags_from(p, ld_flag(p), epoch); }
DI f32x16 mfma32_c(bf16x8 a, bf16x8 b, const f32x16& c) {
  f32x16 d;
  asm volatile("s_nop 1\n\tv_mfma_f32_32x32x16_bf16 %0, %1, %2, %3" : "=&v"(d) : "v"(a), "v"(b), "v"(c));
  return d;
}

DI float dot2(unsigned a, unsigned b, float c) {
  return __builtin_amdgcn_fdot2_f32_bf16(__builtin_bit_cast(bf16x2, a), __builtin_bit_cast(bf16x2, b), c, false);
}
DI int rfl(int v) { return __builtin_amdgcn_readfirstlane(v); }
DI int mini(int a, int b) { return a < b ? a : b; }
typedef __attribute__((ext_vector_type(2))) int i32x2;
typedef __attribute__((ext_vector_type(4))) int i32x4;
template <int I> struct IC { static constexpr int value = I; };
template <int I, int N, class F> DI void static_for(F&& f) { if constexpr (I < N) { f(IC<I>{}); static_for<I + 1, N>(f); } }
#define FENCE() asm volatile("" ::: "memory")
constexpr int SPIN_CAP = 1 << 22;
DI void lds_wait_eq(unsigned v, unsigned addr, unsigned want) {
  unsigned s0, cnt;
  asm volatile("s_mov_b32 %2, 0x400000\n"
               "1:\n\tv_readfirstlane_b32 %1, %0\n\ts_cmp_eq_u32 %1, %4\n\ts_cbranch_scc1 2f\n\t"
               "ds_read_b32 %0, %3\n\ts_sub_u32 %2, %2, 1\n\ts_waitcnt lgkmcnt(0)\n\ts_cbranch_scc0 1b\n"
               "2:"
               : "+v"(v), "=&s"(s0), "=&s"(cnt) : "v"(addr), "s"(want) : "memory", "scc");
}
DI void lds_wait_ge(unsigned addr, unsigned need) {
  unsigned v, s0, cnt;
  asm volatile("s_mov_b32 %2, 0x400000\n"
               "1:\n\tds_read_b32 %0, %3\n\ts_waitcnt lgkmcnt(0)\n\tv_readfirstlane_b32 %1, %0\n\ts_cmp_ge_u32 %1, %4\n\ts_cbranch_scc1 2f\n\t"
               "s_sub_u32 %2, %2, 1\n\ts_cbranch_scc0 1b\n"
               "2:"
               : "=&v"(v), "=&s"(s0), "=&s"(cnt) : "v"(addr), "s"(need) : "memory", "scc");
}
DI void lds_wait_halves(unsigned v, unsigned addr, unsigned need) {
  unsigned s0, s1, cnt;
  asm volatile("s_mov_b32 %3, 0x400000\n"
               "1:\n\tv_readfirstlane_b32 %1, %0\n\ts_lshr_b32 %2, %1, 16\n\ts_and_b32 %1, %1, 0xffff\n\ts_min_u32 %1, %1, %2\n\t"
               "s_cmp_ge_u32 %1, %5\n\ts_cbranch_scc1 2f\n\t"
               "ds_read_b32 %0, %4\n\ts_sub_u32 %3, %3, 1\n\ts_waitcnt lgkmcnt(0)\n\ts_cbranch_scc0 1b\n"
               "2:"
               : "+v"(v), "=&s"(s0), "=&s"(s1), "=&s"(cnt) : "v"(addr), "s"(need) : "memory", "scc");
}

__global__ __launch_bounds__(512, 2) void fused_block(const char* __restrict__ x, const char* __restrict__ wqkv, const char* __restrict__ wo,
                                                      const char* __restrict__ wg, const char* __restrict__ wu, const char* __restrict__ wd,
                                                      char* __restrict__ out, char* ws) {
  __shared__ __attribute__((aligned(16))) char smem_[163840];
  LAS char* sm = (LAS char*)smem_;
  const unsigned sm0 = (unsigned)(uintptr_t)sm;
  const int tid = threadIdx.x, lane = tid & 63;
  const int wid = __builtin_amdgcn_readfirstlane(tid >> 6);
  STAMP(0);
  const int b = blockIdx.x, g = 4 * ((b & 7) >> 1) + (b >> 6), c = 8 * (b & 1) + ((b >> 3) & 7);
  const int wgid = g * 16 + c;
  const int g2 = AMAP == 0 ? g : AMAP == 1 ? (b >> 4) : 8 * (b & 1) + (b >> 5), c2 = AMAP == 0 ? c : AMAP == 1 ? 2 * (b & 7) + ((b >> 3) & 1) : 4 * ((b & 7) >> 1) + ((b >> 3) & 3);
  const int c1 = ((g & 1) && (c & 7) == 7) ? c ^ 8 : c;
  const bool lx = !(wid & 1);
  unsigned vo[8];
  {
    const unsigned rsh = lx ? 3 : 4, rstr = lx ? 1280 : 3840, pstr = (lx ? 8 * 1280 : 4 * 3840) - 1024;
    const unsigned cm = lx ? 7 : 15, csh = lx ? 0 : 2, chx = lx ? 64 : 0;
    const unsigned ch0 = (lane & cm) ^ ((lane >> 4) << csh);
    const unsigned vb = (lane >> rsh) * rstr + 4096;
#pragma unroll
    for (int i = 0; i < 8; ++i) vo[i] = vb + i * pstr + ((ch0 << 4) ^ ((i & 1) ? chx : 0));
  }
  const char* const rsb = (lx ? x : wqkv) + (lx ? g * (128 * 1280) : c1 * 256);
  const unsigned hstep = lx ? 64 * 1280 : 32 * 3840, sstep = lx ? 128 : 64 * 3840, rdst = sm0 + L_P1 + (wid & 1) * 16384;
  const int rot1 = (R1C * (c & 7) + R1G * (g & 3)) % 10;
  if (c1 < 15 && wid < 4) {
#pragma unroll
    for (int s = 0; s < 2; ++s) glds8(vo, rsb + (rot1 + s == 10 ? 0 : rot1 + s) * sstep + (wid >> 1) * hstep, rdst + s * 32768 + (wid >> 1) * 8192);
  }
  SCHED();
  const auto rs = __builtin_amdgcn_make_buffer_rsrc(ws, 0, WS_END, 0x00020000);
  const bool isC = wid < 4, isL = (wid >> 1) == 2;
  const int l31 = lane & 31, hi = lane >> 5, G = lane >> 4, q4 = (lane & 15) >> 2, p4 = lane & 3, l15 = lane & 15;
  STAMP(6);
  u64 eraw = 0, epoch = 0;
  auto load_epoch = [&]() { unsigned z = 0; OPAQUE(z); eraw = ld_flag(ws + WS_EPOCH + z); };
  auto get_epoch = [&]() {
    const u64 e = mix64(eraw);
    const unsigned e0 = __builtin_amdgcn_readfirstlane((unsigned)e), e1 = __builtin_amdgcn_readfirstlane((unsigned)(e >> 32));
    epoch = ((u64)e1 << 32) | e0;
  };
  auto wset = [&](int w, int v) { FENCE(); *(volatile LAS int*)(sm + L_W + w) = v; FENCE(); };
  auto wget = [&](int w) { return rfl(*(volatile LAS int*)(sm + L_W + w)); };
  auto wget4 = [&](int w) { const i32x4 v = *(volatile LAS i32x4*)(sm + L_W + w); return rfl(mini(mini(v[0], v[1]), mini(v[2], v[3]))); };
  auto wwait = [&](int w, int need) { for (int sp = 0; wget(w) < need && sp < SPIN_CAP; ++sp) __builtin_amdgcn_s_sleep(1); FENCE(); };
  auto wraw = [&](int w) { return *(volatile LAS unsigned*)(sm + L_W + w); };
  auto wset16 = [&](int w, int v) { FENCE(); *(volatile LAS short*)(sm + L_W + w) = (short)v; FENCE(); };
  auto wwait2 = [&](int w, unsigned first, int need) { lds_wait_halves(first, sm0 + L_W + w, need); };
  auto count = [&](int k) {
    unsigned old = 0;
    if (lane == 0) old = __hip_atomic_fetch_add((LAS unsigned*)(sm + L_W + W_CNT + 4 * k), 1u, __ATOMIC_RELAXED, __HIP_MEMORY_SCOPE_WORKGROUP);
    return rfl(old);
  };
  auto raise = [&](unsigned off) { if (lane < FLW) st_flag(ws + off + lane * 8, epoch); };
  auto arrive_n = [&](int k, unsigned flag_base, int n) {
    if (count(k) == n - 1) {
      raise(flag_base + (g * 16 + c1) * 128);
      if (k == 1 && c1 == 14) raise(WS_HINT + g * 128);
    }
  };
  if (wid == 7) {
    u64 pc;
    asm volatile("s_getpc_b64 %0" : "=s"(pc));
    unsigned sv;
    const int ln = lane < CODE_LINES ? lane : 0;
    asm volatile("s_mov_b32 %0, m0\n\ts_mov_b32 m0, %3\n\ts_nop 2\n\tglobal_load_lds_dword %1, %2\n\ts_mov_b32 m0, %0"
                 : "=&s"(sv) : "v"((((b >> 3) & 7) * CODE_LINES + ln) * 128), "s"(pc), "s"(sm0 + L_JUNK) : "memory");
  }
  if (wid == 6) *(LAS unsigned*)(sm + L_W + 4 * lane) = 0;
  if (wid == 7) *(LAS u32x2*)(sm + L_ONES + 8 * lane) = u32x2{0x3f803f80u, 0x3f803f80u};
  if (wid == 6 && lane < 12) *(LAS u32x4*)(sm + L_KT + 16 * lane) = u32x4{lane < 8 - 4 * g2 ? 0x3f803f80u : 0x00003f80u, 0, 0, 0};

  auto arrive_w = [&](int k, unsigned flag_base) {
    if (count(k) == 3) raise(flag_base + wgid * 128);
  };
  bf16x8 rf[10];
  unsigned rvo = 0, rvo1 = 0, rstep5 = 0;
  int rn = 0, roct = 0;
  auto wide_read = [&](unsigned st, unsigned dst, unsigned nk5) {
    LAS char* p = sm + st + q4 * 1280 + G * 320;
#pragma unroll
    for (int j = 0; j < 10; ++j) {
      const int tt = j % 5;
      LAS char* pj = p + (j / 5) * 160 + (tt < 4 ? 64 * (tt >> 1) + 16 * p4 + 8 * (tt & 1) : 128 + 8 * p4);
      rf[j] = ld_tr2(pj, pj + 4 * 1280);
    }
    rvo = rvo1 = dst + G * 2 * nk5 * 1024 + l15 * 16; rstep5 = nk5 * 1024; rn = 10;
  };
  auto retile_store = [&](int j) {
    const int o = roct + (j >> 1);
    ST128R(__builtin_bit_cast(u32x4, rf[j]), (j & 1) ? rvo1 : rvo,
          rn == 10 ? (j % 5) * 1024 + (j / 5) * rstep5 : (o >> 1) * 3072 + (o & 1) * 512);
  };
  const int su = wgid * 3 + wid - 5;
  const unsigned rst = L_RP + (wid == 4 ? 0 : 10240 + (wid - 5) * 6144);
  auto late_load = [&](int w) {
    const unsigned rsw = sm0 + L_RP + (w == 4 ? 0 : 10240 + (w - 5) * 6144);
    const int suw = wgid * 3 + w - 5;
    if (w == 4) {
      if (wgid < 192) {
        glds5(lane * 16, wd + (size_t)wgid * 10240, rsw);
        glds5(lane * 16, wd + (size_t)wgid * 10240 + 5120, rsw + 5120);
      }
    } else if (suw < 648) {
      const int cl = suw % 24, rgp = suw / 24;
      const unsigned vr = LP == 0 ? (lane >> 3) * 3072 + (lane & 7) * 16 : LP == 1 ? lane * 16 : (lane >> 5) * 3072 + (lane & 31) * 16;
#pragma unroll
      for (int i = 0; i < 3; ++i) {
        const size_t so = LP == 0 ? (size_t)mini(3 * rgp + i, 79) * 8 * 3072 + cl * 128 : LP == 1 ? (size_t)mini(suw, 639) * 3072 + i * 1024
                          : (size_t)(mini(wgid / 6, 34) * 18 + (w - 5) * 6 + 2 * i) * 3072 + (wgid % 6) * 512;
        glds(vr, wg + so, rsw + i * 1024);
        glds(vr, wu + so, rsw + 3072 + i * 1024);
      }
    }
  };
  auto late_read = [&]() {
    WAITV(0);
    if (wid == 4) {
      const int pos = 96 * ((wgid % 12) / 6) + 6 * (wgid / 12) + wgid % 6;
      if (wgid < 192) wide_read(rst, WS_W5 + (pos >> 2) * 5120 + (pos & 3) * 256, 240);
    } else if (su < 648) {
      const int cl = su % 24, rgp = su / 24;
      float cs[2] = {0.f, 0.f};
      rn = rgp == 26 ? 4 : 6;
#pragma unroll
      for (int j = 0; j < 6; ++j) {
        LAS char* p = sm + rst + (G & 1) * 3072 + ((j >> 1) * 8 + q4) * 128 + (2 * (j & 1) + hi) * 32 + 16 * (p4 & 1) + 8 * (p4 >> 1);
        rf[j] = ld_tr2(p, p + 4 * 128);
        const u32x4 w = __builtin_bit_cast(u32x4, rf[j]);
        const unsigned one = j < rn ? 0x3f803f80u : 0u;
#pragma unroll
        for (int d = 0; d < 4; ++d) cs[j & 1] = dot2(w[d], one, cs[j & 1]);
      }
      {
        const unsigned t0 = 4 * cl + hi, t1 = t0 + 2;
        rvo = WS_W4 + ((t0 / 3) * 120 + t0 % 3) * 1024 + l31 * 16;
        rvo1 = WS_W4 + ((t1 / 3) * 120 + t1 % 3) * 1024 + l31 * 16;
      }
      roct = 3 * rgp;
      const unsigned cvo = lane * 4;
#pragma unroll
      for (int jp = 0; jp < 2; ++jp)
        __builtin_amdgcn_raw_buffer_store_b32(__float_as_uint(cs[jp]), rs, cvo + WS_CSUM + (unsigned)(rgp * 12288 + cl * 512 + jp * 256), 0, 16);
    }
    FENCE();
    if (lane == 0) __hip_atomic_fetch_add((LAS unsigned*)(sm + L_W + W_S1DONE), 1u, __ATOMIC_RELAXED, __HIP_MEMORY_SCOPE_WORKGROUP);
    FENCE();
  };
  if (isL && c1 < 15) {
    unsigned vx[8], vw[8];
#pragma unroll
    for (int i = 0; i < 8; ++i) {
      vx[i] = (lane >> 3) * 1280 + 4096 + i * (8 * 1280 - 1024) + ((((lane & 7) ^ (lane >> 4)) << 4) ^ ((i & 1) * 64));
      vw[i] = (lane >> 4) * 3840 + 4096 + i * (4 * 3840 - 1024) + (((lane & 15) ^ ((lane >> 4) << 2)) << 4);
    }
    int s = 2 + (wid & 1);
    BAR();
    if (LP1) __builtin_amdgcn_s_setprio(LP1);
#pragma unroll 1
    for (; s < 10; s += 2) {
      const int kb = rot1 + s >= 10 ? rot1 + s - 10 : rot1 + s;
      const char* px = x + g * (128 * 1280) + kb * 128;
      const char* pw = wqkv + c1 * 256 + kb * (64 * 3840);
      if (PB && s >= PB) { WAITV(0); wset(W_INIT + 4 * (s - 2), 0x01010101); }
      for (int sp = 0; wget4(W_CONS1) < s - 3 && sp < SPIN_CAP; ++sp) { if (LSL) __builtin_amdgcn_s_sleep(1); }
      STAMP2(7 + s / 2);
      FENCE();
      const unsigned d = sm0 + L_P1 + (s & 3) * 32768;
      glds8(vx, px, d);
      glds8(vw, pw, d + 16384);
      WAITV(16);
      if (s >= 4) wset(W_INIT + 4 * (s - 2), 0x01010101);
      STAMP(16 + s - 2);
      glds8(vx, px + 64 * 1280, d + 8192);
      glds8(vw, pw + 32 * 3840, d + 24576);
    }
    WAITV(0);
    wset(W_INIT + 4 * (s - 2), 0x01010101);
    STAMP(24);
    if (LP1) __builtin_amdgcn_s_setprio(0);
    load_epoch();
    if (wid == 4 ? !(EV & 4) : !(EV & 2)) late_load(wid);
    get_epoch();
    if (wid == 4 && (EV & 4)) wwait(W_STATS, 1);
    if (wid == 5 && (EV & 2)) wwait(W_LL, 1);
    late_read();
  } else if (isC && c1 < 15) {
    const int wn = wid & 1, wt = wid >> 1;
    f32x16 acc[2][2] = {};
    const unsigned ax = L_P1 + (64 * wt + l31) * 128 + ((hi ^ ((l31 >> 1) & 7)) << 4);
    const unsigned aw = L_P1 + 16384 + (8 * hi + q4) * 256 + (((8 * wn + 2 * (G & 1) + (p4 & 1)) ^ (q4 << 2)) << 4) + 8 * (p4 >> 1);
    bf16x8 xf[4][2], wf[4][2];
    auto rd = [&](int ks, unsigned so) {
#pragma unroll
      for (int t = 0; t < 2; ++t) {
        wf[ks][t] = ld_tr2(sm + (((aw ^ (t * 64)) + so) + ks * 4096), sm + (((aw ^ (t * 64)) + so) + ks * 4096 + 1024));
        xf[ks][t] = ld_b128(sm + (((ax ^ (ks * 32)) + so) + t * 4096));
      }
    };
    auto wset8 = [&](int w) { FENCE(); *(volatile LAS char*)(sm + L_W + w) = 1; FENCE(); };
    BAR();
    WAITV(8);
    wset8(W_INIT + wid);
    lds_wait_eq(0u, sm0 + L_W + W_INIT, 0x01010101u);
    STAMP(16);
    rd(0, 0); rd(1, 0);
    if (LA == 3) rd(2, 0);
    unsigned lv = 0;
    SCHED();
#pragma unroll 1
    for (int s = 0; s < 10; ++s) {
      const unsigned so = (s & 3) * 32768, sn = ((s + 1) & 3) * 32768;
#pragma unroll
      for (int ks = 0; ks < 4; ++ks) {
        const int wn1 = W_INIT + 4 * (s < 9 ? s + 1 : 9);
        if (LA == 2 && ks == 0) lv = wraw(wn1);
        if (LA == 3 && ks == 3) lv = wraw(W_INIT + 4 * (s < 8 ? s + 2 : 9));
        if (ks == 4 - LA) {
          if (s == 0) { WAITV(0); wset8(wn1 + wid); }
          lds_wait_eq(lv, sm0 + L_W + wn1, 0x01010101u);
          STAMP2(17 + s);
          SCHED();
        }
        if (ks < 4 - LA) rd(ks + LA, so); else rd(ks + LA - 4, sn);
        if (ks == 3 - LA) wset(W_CONS1 + 4 * wid, s + 1);
#pragma unroll
        for (int tj = 0; tj < 2; ++tj)
#pragma unroll
          for (int ti = 0; ti < 2; ++ti) acc[tj][ti] = MFMA32(wf[ks][tj], xf[ks][ti], acc[tj][ti]);
        SGB(0x8, 1); SGB(0x100, 2); SGB(0x8, 1); SGB(0x100, 2); SGB(0x8, 1); SGB(0x100, 2); SGB(0x8, 1);
        SCHED();
      }
    }
    STAMP(1);
    wwait(W_STATS, 1);
    {
      const u32x2 e = *(volatile LAS u32x2*)(sm + L_W + W_EPO);
      epoch = ((u64)(unsigned)rfl(e[1]) << 32) | (unsigned)rfl(e[0]);
    }
    const float sc = (c1 < 5) ? QSCALE : 1.f;
#pragma unroll
    for (int ti = 0; ti < 2; ++ti) {
      const int tok = 64 * wt + 32 * ti + l31;
      const f32x2 st = *(LAS f32x2*)(sm + L_STAT + tok * 8);
      const float r = st[1] * sc, nm = -st[0];
#pragma unroll
      for (int tj = 0; tj < 2; ++tj)
#pragma unroll
        for (int hf = 0; hf < 2; ++hf) {
          const int col = 64 * wn + 32 * tj + 16 * hf + 8 * hi;
          const f32x4 ca = *(LAS f32x4*)(sm + L_CS + col * 4), cb = *(LAS f32x4*)(sm + L_CS + col * 4 + 16);
          float v[8];
#pragma unroll
          for (int j = 0; j < 4; ++j) {
            v[j] = (acc[tj][ti][8 * hf + j] + nm * ca[j]) * r;
            v[4 + j] = (acc[tj][ti][8 * hf + 4 + j] + nm * cb[j]) * r;
          }
          u32x4 o = {pk2(v[0], v[1]), pk2(v[2], v[3]), pk2(v[4], v[5]), pk2(v[6], v[7])};
          ST128(o, hi * 2048 + tok * 16, WS_QKV + (unsigned)(g * 240 + 16 * c1 + 8 * wn + 4 * tj + 2 * hf) * 2048);
        }
    }
    WAITV(0);
    arrive_n(1, WS_F1, 4);
  } else {
    const int sw = wid & 1;
    const bool srv = wid >= 6;
    load_epoch();
    u32x4 v[10];
    if (srv) {
      const char* src = sw ? wqkv + (size_t)(40 * g) * 3840 + (c1 < 15 ? c1 : 0) * 256 : x + (size_t)(128 * g + 8 * c1) * 1280;
      const unsigned vs = sw ? G * 3840 + l15 * 16 : (lane >> 3) * 1280 + (lane & 7) * 16, st = sw ? 4 * 3840 : 128;
#pragma unroll
      for (int i = 0; i < 10; ++i) v[i] = NT2 ? __builtin_nontemporal_load((const u32x4*)(src + i * st + vs)) : *(const u32x4*)(src + i * st + vs);
    }
    SCHED();
    BAR();
    if (srv) {
      STAMP(15);
      if (sw == 0) {
        float s = 0.f, q = 0.f;
#pragma unroll
        for (int i = 0; i < 10; ++i)
#pragma unroll
          for (int j = 0; j < 4; ++j) { s = dot2(v[i][j], 0x3f803f80u, s); q = dot2(v[i][j], v[i][j], q); }
        s = sum8(s); q = sum8(q);
        const float mu = s * (1.f / 640.f);
        const float rstd = __builtin_amdgcn_rsqf(fmaxf(q * (1.f / 640.f) - mu * mu, 0.f) + 1e-5f);
        if ((lane & 3) == 0) ST64((u32x2{__float_as_uint(mu), __float_as_uint(rstd)}), (lane & 56) + (lane & 4) * 16, WS_ST1 + (unsigned)(16 * g + c1) * 128);
      } else {
        float a[8] = {};
#pragma unroll
        for (int i = 0; i < 10; ++i)
#pragma unroll
          for (int j = 0; j < 4; ++j) { a[2 * j] += bflo(v[i][j]); a[2 * j + 1] += bfhi(v[i][j]); }
#pragma unroll
        for (int j = 0; j < 8; ++j) a[j] = sum32(sum16(a[j]));
        if (lane < 32) {
          const f32x4 o = (lane & 16) ? f32x4{a[4], a[5], a[6], a[7]} : f32x4{a[0], a[1], a[2], a[3]};
          __builtin_amdgcn_raw_buffer_store_b128(__builtin_bit_cast(u32x4, o), rs, l15 * 32 + (lane & 16) + WS_CS1 + (unsigned)(c1 * 16 + g) * 512, 0, 16 | NT6);
        }
      }
      get_epoch();
      WAITV(0);
      arrive_n(0, WS_F0, 2);
      STAMP(16);
    } else {
      get_epoch();
      if (wid < 5) {
        const int o = 5 * g + wid;
        const unsigned stg = L_HSTG + wid * 10240;
        glds5(lane * 16, wo + (size_t)o * 10240, sm0 + stg);
        glds5(lane * 16, wo + (size_t)o * 10240 + 5120, sm0 + stg + 5120);
        WAITV(0);
        const int pos = wid == 2 ? 64 + g : 4 * g + wid - (wid > 2);
        wide_read(stg, WS_W3 + (pos >> 2) * 5120 + (pos & 3) * 256, 100);
#pragma unroll
        for (int j = 0; j < 10; ++j)
          __builtin_amdgcn_raw_buffer_store_b128(__builtin_bit_cast(u32x4, rf[j]), rs, rvo + (j % 5) * 1024 + (j / 5) * rstep5, 0, 16 | NT5);
        rn = 0;
        WAITV(0);
        arrive_n(1, WS_F1, 5);
      }
    }
    if (wid == 7 && c1 < 15) {
      if (EV & 1) late_load(7);
      if (EV & 4) late_load(4);
      __builtin_amdgcn_s_sleep(FDLY);
      wait_flags(ws + WS_F0 + (lane < 16 ? g * 16 + lane : (l15 * 16 + c1)) * 128, epoch);
      const u32x4 gs = LD128((lane >> 2) * 128 + p4 * 16, WS_ST1 + (unsigned)g * 2048);
      f32x4 t = {};
#pragma unroll
      for (int i = 0; i < 8; ++i) t += __builtin_bit_cast(f32x4, LD128(hi * 4096 + l31 * 16, WS_CS1 + (unsigned)(c1 * 16 + i) * 512));
#pragma unroll
      for (int j = 0; j < 4; ++j) t[j] = sum32(t[j]);
      *(LAS u32x4*)(sm + L_STAT + lane * 16) = gs;
      if (lane < 32) *(LAS f32x4*)(sm + L_CS + lane * 16) = t;
      *(volatile LAS u32x2*)(sm + L_W + W_EPO) = u32x2{(unsigned)epoch, (unsigned)(epoch >> 32)};
      wset(W_STATS, 1);
      STAMP(19);
    }
    if (wid >= 4) {
      if (!((EV & 1) && wid == 7 && c1 < 15)) late_load(wid);
      if ((EV & 2) && wid == 6 && c1 < 15) { late_load(5); WAITV(0); wset(W_LL, 1); }
      late_read();
    }
    STAMP(17);
    if (wid == 6) {
      if (c1 < 15) { for (int sp = 0; wget(W_CNT + 4) < 4 && sp < SPIN_CAP; ++sp) __builtin_amdgcn_s_sleep(1); __builtin_amdgcn_s_sleep(GDLY); }
      else wait_flags(ws + WS_HINT + g * 128, epoch);
      wwait(W_S1DONE, 4);
      const int i = lane < 18 ? lane : 0;
      const int which = i / 6, po = which ? (i % 6) >> 1 : 0, e = i & 1;
      const int tile = (640 * which + 40 * c2 + 39 * e) >> 7;
      const int pn = g2 - po < 0 ? 0 : g2 - po;
      wait_flags(ws + WS_F1 + (pn * 16 + tile) * 128, epoch);
      wset(W_GO, 1);
    }
  }
  const char* wsrc3 = ws + WS_W3 + (c & 7) * 102400 + (wid & 1) * 10240;
  const char* wsrc4 = ws + WS_W4 + c * 245760 + (wid & 1) * 10240 - 5 * 20480;
  const char* wsrc5 = ws + WS_W5 + (c & 7) * 245760 + (wid & 1) * 10240 - 17 * 20480;
  auto wissue_half = [&](int st, int h) {
    const char* sb = (st < 5 ? wsrc3 : st < 17 ? wsrc4 : wsrc5) + st * 20480 + h * 5120;
    glds5_sc1(lane * 16, sb, sm0 + L_RING + ((st + 3) % 7) * RSLOT + (wid & 1) * 10240 + h * 5120);
  };
  STAMP(2);

  bool wready;
  {
    const int hs = wid >> 2, qb = wid & 3;
    if (!isC) {
      const int bk = wid & 3, pn = (bk == 3 || g2 - bk < 0) ? g2 : g2 - bk;
      const char* sA = ws + WS_QKV + (size_t)(pn * 240 + (bk == 3 ? 0 : 80) + 5 * c2) * 2048;
      const unsigned dA = sm0 + (bk == 3 ? L_OST : L_K + (2 - bk) * 2048), dstr = bk == 3 ? 2048 : 6144, dB = sm0 + L_V + (2 - bk) * 2048;
      if (wid == 7) {
        if (c1 == 15) wait_flags(ws + WS_F1 + (l15 * 16 + 15) * 128, epoch);
        if (__all(ld_flag(ws + WS_F1 + (l15 * 16 + 15) * 128) == epoch)) wset(W_WREADY, 1);
      }
      wwait(W_GO, 1);
      STAMP(3);
#pragma unroll
      for (int j = 0; j < 5; ++j) glds2_sc1(lane * 16, sA + j * 2048, dA + j * dstr);
      if (bk < 3) {
#pragma unroll
        for (int j = 0; j < 5; ++j) glds2_sc1(lane * 16, sA + (80 + j) * 2048, dB + j * VSTR);
      }
    }
    const unsigned ak0 = L_K + (3 * hs + hi) * 6144 + l31 * 16, ak1 = L_K + 2 * 6144 + l31 * 16;
    const int kb_lo = (g2 >= 2) ? qb : (g2 == 1 ? 4 : 8), kb_hi = qb + 8;
    const float NINF = -__builtin_inff();
    f32x16 cD, cF;
#pragma unroll
    for (int r = 0; r < 16; ++r) {
      const bool low = (r & 3) + 8 * (r >> 2) + 4 * hi <= l31;
      cD[r] = low ? 0.f : NINF;
      cF[r] = low ? NINF : 0.f;
    }
    const bool isOne = (G & 1) && p4;
    const unsigned av0 = isOne ? L_ONES
                               : L_V + (4 * hi + q4) * 16 + ((G & 1) ? 2 * VSTR + 8 * hs : (3 * hs + (p4 & 1)) * VSTR + 8 * (p4 >> 1));
    const unsigned avs = isOne ? 0 : 512;
    unsigned a0 = ak0 + qb * 512, a1 = hi ? L_KT + qb * 16 : ak1 + qb * 512, av = av0 + qb * avs;
    const unsigned a1s = hi ? 16 : 512;
    asm volatile("" : "+v"(cD), "+v"(cF), "+v"(a0), "+v"(a1), "+v"(av));
    if (wid >= 4 && wid < 7) WAITV(10); else WAITV(0);
    BAR();
    STAMP(4);
    wready = wget(W_WREADY);
    const bool wiss = wready && (wid >> 1) == WIW;
    const int row = 32 * qb + l31;
    u32x4 q0 = *(LAS u32x4*)(sm + L_OST + (3 * hs + hi) * 2048 + row * 16), q1 = *(LAS u32x4*)(sm + L_OST + 2 * 2048 + row * 16);
    if (hi || hs) { q1[0] = 0; q1[1] = 0; }
    if (hi || !hs) { q1[2] = 0; q1[3] = 0; }
    const bf16x8 qf0 = __builtin_bit_cast(bf16x8, q0), qf1 = __builtin_bit_cast(bf16x8, q1);
    auto vmax = [&](const f32x16& s, float m) {
#pragma unroll
      for (int r = 0; r < 16; r += 2) m = fmaxf(fmaxf(m, s[r]), s[r + 1]);
      return m;
    };
    f32x16 o;
    float rl, m;
    auto finish = [&]() {
      auto sw = __builtin_amdgcn_permlane32_swap(__float_as_uint(o[8]), __float_as_uint(o[8]), false, false);
      const float l = __uint_as_float(sw[1]);
      rl = __builtin_amdgcn_rcpf(l);
      float am = 0.f;
#pragma unroll
      for (int r = 0; r < 12; r += 2) am = fmaxf(fmaxf(am, fabsf(o[r])), fabsf(o[r + 1]));
      return (bool)__all(l <= 0x1p30f && l >= 0x1p-30f && am < __builtin_inff());
    };
    {
      f32x16 s[2];
      bf16x8 k0 = ld_b128(sm + a0 + 8 * 512), k1 = ld_b128(sm + a1 + 8 * a1s);
      s[1] = mfma32_c(k0, qf0, cD);
      s[0] = mfma32_c(k1, qf1, s[1]);
      asm volatile("s_nop 11" : "+v"(s[0]));
      m = max32(vmax(s[0], NINF));
      u32x4 q2 = q1;
      if (hi) q2[0] = pk2(-m, -3.0e38f);
      const bf16x8 qf2 = __builtin_bit_cast(bf16x8, q2);
      s[0] = MFMA32(k1, qf2, s[1]);
      k0 = ld_b128(sm + a0); k1 = ld_b128(sm + a1);
      o = f32x16{};
      SCHED();
      if (RS == 1 && rn) { retile_store(0); retile_store(1); WAITV(2); }
      else if (RS == 2 && rn) { retile_store(0); retile_store(1); retile_store(2); retile_store(3); WAITV(4); }
      else WAITV(0);
      BAR();
      SCHED();
#pragma unroll
      for (int j = 0; j < 9; ++j) {
        if (APRIO == 1) { if (((j + hs) & 1)) __builtin_amdgcn_s_setprio(1); else __builtin_amdgcn_s_setprio(0); }
        if (APRIO == 2 && j == 0 && hs) __builtin_amdgcn_s_setprio(1);
        if (APRIO == 2 && j == AJ && hs) __builtin_amdgcn_s_setprio(0);
        f32x16& sc = s[j & 1];
        f32x16& sn = s[(j + 1) & 1];
        unsigned avj = av + (j == 0 ? 8 : j - 1) * avs;
        bf16x8 vf0 = ld_tr2(sm + avj, sm + avj + 128), vf1 = ld_tr2(sm + avj + 256, sm + avj + 384);
        unsigned pw[8];
        if (j < 8) sn = MFMA32(k0, qf0, j == 0 ? cF : f32x16{});
        SCHED();
#pragma unroll
        for (int r = 0; r < 2; ++r) pw[r] = pk2(__builtin_amdgcn_exp2f(sc[2 * r]), __builtin_amdgcn_exp2f(sc[2 * r + 1]));
        SCHED();
        if (j < 8) sn = MFMA32(k1, qf2, sn);
        if (j < 7) { a1 += a1s; k0 = ld_b128(sm + a0 + (j + 1) * 512); k1 = ld_b128(sm + a1); }
        SCHED();
#pragma unroll
        for (int r = 2; r < 5; ++r) pw[r] = pk2(__builtin_amdgcn_exp2f(sc[2 * r]), __builtin_amdgcn_exp2f(sc[2 * r + 1]));
        SCHED();
        o = MFMA32(vf0, __builtin_bit_cast(bf16x8, (u32x4{pw[0], pw[1], pw[2], pw[3]})), o);
        SCHED();
#pragma unroll
        for (int r = 5; r < 8; ++r) pw[r] = pk2(__builtin_amdgcn_exp2f(sc[2 * r]), __builtin_amdgcn_exp2f(sc[2 * r + 1]));
        SCHED();
        o = MFMA32(vf1, __builtin_bit_cast(bf16x8, (u32x4{pw[4], pw[5], pw[6], pw[7]})), o);
        SCHED();
        if (RS == 0 && j + RE < 5 && 2 * (j + RE) < rn) { retile_store(2 * (j + RE)); retile_store(2 * (j + RE) + 1); }
        if (RS == 1 && j < 2 && 4 * j + 2 < rn) { retile_store(4 * j + 2); retile_store(4 * j + 3); }
        if (RS == 1 && j < 2 && 4 * j + 4 < rn) { retile_store(4 * j + 4); retile_store(4 * j + 5); }
        if (RS == 2 && j < 3 && 2 * j + 4 < rn) { retile_store(2 * j + 4); retile_store(2 * j + 5); }
        if (wiss) {
#pragma unroll
          for (int h = 0; h < 8; ++h)
            if (h * WJ / 8 == j) wissue_half(h >> 1, h & 1);
        }
      }
    }
    if (APRIO) __builtin_amdgcn_s_setprio(0);
    if (!finish()) {
      int lq = l31 - 4 * hi;
      OPAQUE(lq);
#pragma unroll 1
      for (int k2 = kb_lo; k2 < kb_hi; ++k2) {
        f32x16 cz;
#pragma unroll
        for (int r = 0; r < 16; ++r) cz[r] = (k2 == qb && (r & 3) + 8 * (r >> 2) <= lq) ? NINF : 0.f;
        f32x16 s = MFMA32(ld_b128(sm + ak0 + k2 * 512), qf0, cz);
        s = MFMA32(ld_b128(sm + ak1 + k2 * 512), qf1, s);
        m = vmax(s, m);
      }
      m = max32(m);
      o = f32x16{};
#pragma unroll 1
      for (int kb = kb_lo; kb <= kb_hi; ++kb) {
        f32x16 cin;
#pragma unroll
        for (int r = 0; r < 16; ++r) cin[r] = ((r & 3) + 8 * (r >> 2) <= lq ? kb == qb : kb == kb_hi) ? NINF : -m;
        f32x16 s = MFMA32(ld_b128(sm + ak0 + kb * 512), qf0, cin);
        s = MFMA32(ld_b128(sm + ak1 + kb * 512), qf1, s);
        const unsigned av = av0 + kb * avs;
        bf16x8 vf0 = ld_tr2(sm + av, sm + av + 128), vf1 = ld_tr2(sm + av + 256, sm + av + 384);
        unsigned pw[8];
#pragma unroll
        for (int r = 0; r < 8; ++r) pw[r] = pk2(__builtin_amdgcn_exp2f(s[2 * r]), __builtin_amdgcn_exp2f(s[2 * r + 1]));
        o = MFMA32(vf0, __builtin_bit_cast(bf16x8, (u32x4{pw[0], pw[1], pw[2], pw[3]})), o);
        o = MFMA32(vf1, __builtin_bit_cast(bf16x8, (u32x4{pw[4], pw[5], pw[6], pw[7]})), o);
      }
      (void)finish();
    }
    const unsigned ob = WS_O + (unsigned)(g2 * 80) * 2048;
    ST128((u32x4{pk2(o[0] * rl, o[1] * rl), pk2(o[2] * rl, o[3] * rl), pk2(o[4] * rl, o[5] * rl), pk2(o[6] * rl, o[7] * rl)}),
          hi * 2048 + row * 16, ob + (4 * c2 + 2 * hs) * 2048);
    if (!hi) ST64((u32x2{pk2(o[8] * rl, o[9] * rl), pk2(o[10] * rl, o[11] * rl)}), row * 8, ob + (64 + c2) * 2048 + hs * 1024);
    STAMP(5);
    WAITV(0);
    if (count(3) == 7) {
      raise(WS_F2 + (g2 * 16 + c2) * 128);
      if (lane == 0) st_flag(ws + WS_F1P + wgid * 8, epoch);
    }
    STAMP(6);
    if (isC) wwait(W_CNT + 12, 8);
  }

  if (isL) {
    int issued = 4;
    if (!wready) {
#pragma unroll 1
      for (int i = 0; i < 1; ++i) wait_flags(ws + WS_F1 + (l15 * 16 + 15) * 128, epoch);
      issued = 0;
    }
    const int wl = W_LANDW + 2 * (wid & 1);
    wwait(W_CNT + 12, 8);
    if (wready) wset16(wl, 4);
    for (; issued < 5; ++issued) { const int st = rfl(issued); wissue_half(st, 0); wissue_half(st, 1); }
    WAITV(0);
    wset16(wl, 5);
    __builtin_amdgcn_s_sleep(LDLY);
    {
      u64 f[4];
#pragma unroll
      for (int i = 0; i < 4; ++i) f[i] = ld_flag(ws + WS_F1P + i * 512 + lane * 8);
#pragma unroll
      for (int i = 0; i < 4; ++i) wait_flags_from(ws + WS_F1P + i * 512 + lane * 8, f[i], epoch);
    }
    STAMP(30);
    if (wid == 4) wset(W_GO, 2);
    if (LP2) __builtin_amdgcn_s_setprio(LP2);
    const char* p = wsrc4 + 5 * 20480;
    unsigned dst = sm0 + L_RING + RSLOT + (wid & 1) * 10240;
#pragma unroll 1
    for (int st = 5; st < 29; ++st) {
      if (st == 7 + P3REL) { WAITV(0); wset16(wl, st); }
      for (int sp = 0; wget4(W_CONSW) < (st < 7 ? (P3LATE == 3 ? st - 4 : 1) : st - 5) && sp < SPIN_CAP; ++sp) __builtin_amdgcn_s_sleep(1);
      FENCE();
      glds10_sc1(lane * 16, p, dst);
      p = st == 16 ? wsrc5 + 17 * 20480 : p + 20480;
      dst = dst + RSLOT >= sm0 + L_RING + 7 * RSLOT ? dst - 6 * RSLOT : dst + RSLOT;
      WAITV(20);
      if (st >= 7 && (st < 7 + P3REL || st > 8 + P3REL)) wset16(wl, st - 1);
      STAMP(31 + st - 1);
    }
    WAITV(10);
    wset16(wl, 28);
    WAITV(0);
    wset16(wl, 29);
    STAMP(60);
    STAMP_DUMP();
    return;
  }
  if (!isC) {
    if (wid == 6) {
#pragma unroll 1
      for (int k = 0; k < 2; ++k) {
        for (int sp = 0; (k < 1 ? wget4(W_DONE) <= k : wget(W_CNT + 20) < 4) && sp < SPIN_CAP; ++sp) __builtin_amdgcn_s_sleep(2);
        __builtin_amdgcn_s_sleep(PDLY);
        wait_flags(ws + WS_F4 + k * FL + (g * 16 + l15) * 128, epoch);
        wset(W_RDY, k + 3);
        STAMP(22 + k);
      }
    } else {
      for (int sp = 0; wget(W_GO) < 2 && sp < SPIN_CAP; ++sp) __builtin_amdgcn_s_sleep(4);
      for (int sp = 0; wget4(W_CONSW) < TCS && sp < SPIN_CAP; ++sp) __builtin_amdgcn_s_sleep(4);
      FENCE();
      if (lane < 48) {
        f32x4 t = {};
#pragma unroll
        for (int i = 0; i < 27; ++i) t += __builtin_bit_cast(f32x4, LD128(lane * 16, WS_CSUM + (unsigned)(i * 12288 + c * 768)));
#pragma unroll
        for (int e = 0; e < 4; ++e) {
          const unsigned ch = pk2(t[e], t[e]);
          *(LAS u32x4*)(sm + L_WX + lane * 64 + e * 16) = u32x4{ch, pk2(t[e] - bflo(ch), 0.f), 0, 0};
        }
      }
      wset(W_WREADY, 2);
#pragma unroll 1
      for (int k = 0; k < 1; ++k) {
        for (int sp = 0; wget4(W_DONE) <= k && sp < SPIN_CAP; ++sp) __builtin_amdgcn_s_sleep(1);
        FENCE();
        raise(WS_F4 + k * FL + wgid * 128);
        STAMP(22 + k);
      }
      if (b == 0) {
#pragma unroll 1
        for (int i = 0; i < 4; ++i) wait_flags(ws + WS_F2 + (i * 64 + lane) * 128, epoch);
        if (lane == 0) st_flag(ws + WS_EPOCH, epoch);
      }
    }
    STAMP_DUMP();
    return;
  }

  const int wdn = W_DONE + 4 * wid;
  const int wc = W_CONSW + 4 * wid;
  const unsigned lane16 = lane * 16;

  const int r3 = 64 * (c >> 3) + 16 * wid + l15;
  {
    unsigned hpk[10];
    const char* xp = x + (size_t)(128 * g + r3) * 1280 + (c & 7) * 160;
    const u32x4 xr0 = *(const u32x4*)(xp + G * 16), xr1 = *(const u32x4*)(xp + 64 + G * 16);
    const u32x2 xr2 = *(const u32x2*)(xp + 128 + G * 8);
    u32x4 xf[20];
    auto ldo = [&](int s) {
      const unsigned so = WS_O + (unsigned)(g * 80 + 4 * s) * 2048;
      if (s < 16) xf[s] = LD128(G * 2048 + r3 * 16, so);
      else { const u32x2 a = LD64(G * 2048 + r3 * 8, so), d = LD64(G * 2048 + r3 * 8 + 1024, so); xf[s] = u32x4{a[0], a[1], d[0], d[1]}; }
    };
    f32x4 acc[5] = {};
    {
      constexpr int NB = 15;
      bf16x8 wf[NB];
      unsigned so = 0, lv = 0;
      auto rd = [&](int f) {
        if (f == 80) wwait2(W_LANDW, lv, 5);
        if (f % 20 == 0) { so = lane16; OPAQUE(so); so += slot_of(f / 20); OPAQUE(so); }
        wf[f % NB] = ld_b128(sm + so + (f % 20) * 1024);
        if (f == 64) lv = wraw(W_LANDW);
      };
      __builtin_amdgcn_s_sleep(PDLY - 4);
      wwait2(W_LANDW, 0u, 4);
#pragma unroll
      for (int f = 0; f < NB; ++f) rd(f);
      SCHED();
      wait_flags(ws + WS_F2 + (g * 16 + l15) * 128, epoch);
      STAMP(7);
#pragma unroll
      for (int s = 0; s < XW3; ++s) ldo(s);
      SCHED();
#pragma unroll
      for (int f = 0; f < 100; ++f) {
        acc[f % 5] = MFMA16(wf[f % NB], __builtin_bit_cast(bf16x8, xf[f / 5]), acc[f % 5]);
        SCHED();
        if (f + NB < 100) rd(f + NB);
        if (f % X3S == 0 && XW3 + f / X3S < 20) ldo(XW3 + f / X3S);
        if (P3LATE != 1 && f == 3 * (20 - XW3)) wset(wc, P3LATE ? 1 : 1 + P3REL);
        SCHED();
      }
    }
    STAMP(8);
    float s1 = 0.f, s2 = 0.f;
#pragma unroll
    for (int d = 0; d < 10; ++d) {
      const unsigned xv = d < 4 ? xr0[d & 3] : d < 8 ? xr1[d & 3] : xr2[d & 1];
      const unsigned a = pk2(acc[d >> 1][2 * (d & 1)], acc[d >> 1][2 * (d & 1) + 1]);
      const unsigned h = pk2(bflo(a) + bflo(xv), bfhi(a) + bfhi(xv));
      hpk[d] = h;
      s1 += bflo(h) + bfhi(h);
      s2 += bflo(h) * bflo(h) + bfhi(h) * bfhi(h);
    }
    const unsigned hb = WS_H + (unsigned)(g * 80 + 10 * (c & 7)) * 2048;
    ST128((u32x4{hpk[0], hpk[1], hpk[2], hpk[3]}), G * 2048 + r3 * 16, hb);
    ST128((u32x4{hpk[4], hpk[5], hpk[6], hpk[7]}), G * 2048 + r3 * 16, hb + 4 * 2048);
    {
      auto a0 = __builtin_amdgcn_permlane16_swap(hpk[8], hpk[8], false, false);
      auto a1 = __builtin_amdgcn_permlane16_swap(hpk[9], hpk[9], false, false);
      if (!(G & 1)) ST128((u32x4{a0[0], a1[0], a0[1], a1[1]}), (G >> 1) * 2048 + r3 * 16, hb + 8 * 2048);
    }
    s1 = sum32(sum16(s1)); s2 = sum32(sum16(s2));
    if (G == 0) ST64((u32x2{__float_as_uint(s1), __float_as_uint(s2)}), r3 * 8, WS_STAT + (unsigned)(g * 8 + (c & 7)) * 1024);
    if (P3LATE) wset(wc, 1 + P3REL);
    *(LAS u32x4*)(sm + L_OST + wid * 2560 + lane * 16) = u32x4{hpk[0], hpk[1], hpk[2], hpk[3]};
    *(LAS u32x4*)(sm + L_OST + wid * 2560 + 1024 + lane * 16) = u32x4{hpk[4], hpk[5], hpk[6], hpk[7]};
    *(LAS u32x2*)(sm + L_OST + wid * 2560 + 2048 + lane * 8) = u32x2{hpk[8], hpk[9]};
    WAITV(0);
    arrive_w(4, WS_F3);
    STAMP(9);
  }

  {
    constexpr int NB = 6;
    bf16x8 wf[NB];
    unsigned so = 0;
    unsigned lv = 0;
    auto rd = [&](int n) __attribute__((always_inline)) {
      const int part = n / 123, i = n % 123, f = 120 * part + i;
      if (i >= 120) {
        if (n == 120) lds_wait_ge(sm0 + L_W + W_WREADY, 2);
        wf[n % NB] = ld_b128(sm + L_WX + (3 * part + i - 120) * 512 + (lane16 & 511));
      } else {
      if (f % 20 == 0) { wwait2(W_LANDW, lv, 5 + f / 20 + 1); so = lane16; OPAQUE(so); so += slot_of(5 + f / 20); OPAQUE(so); }
      wf[n % NB] = ld_b128(sm + so + (f % 20) * 1024);
      if (f % 20 == 12) lv = wraw(W_LANDW);
      if (f % 20 == 19 && f / 20 < 12 - P4H) wset(wc, 5 + f / 20 + 2);
      }
    };
    __builtin_amdgcn_s_sleep(PDLY - 2);
#pragma unroll
    for (int n = 0; n < NB; ++n) rd(n);
    SCHED();
    wait_flags(ws + WS_F3 + (g * 16 + l15) * 128, epoch);
    STAMP(10);
    const int r4 = 32 * wid + l31;
    const unsigned vx = hi * 2048 + r4 * 16;
    const unsigned hb = WS_H + (unsigned)(g * 80) * 2048;
    u32x4 xf[40];
    u32x2 stp[4];
    constexpr int XW = XW4;
#pragma unroll
    for (int i = 0; i < XW; ++i) xf[i] = LD128(vx, hb + i * 4096);
    SCHED();
    float r2 = 0.f;
    unsigned xe0 = 0, xe1 = 0;
    static_for<0, 2>([&](auto pc) __attribute__((always_inline)) {
      constexpr int part = pc.value;
      f32x16 acc[3] = {};
      static_for<0, 123>([&](auto ic) __attribute__((always_inline)) {
        constexpr int i = ic.value;
        const int n = 123 * part + i;
        if (i % 20 == 0 && n > 0 && i < 120) { STAMP2(42 + (120 * part + i) / 20); SCHED(); }
        acc[i % 3] = MFMA32(wf[n % NB], __builtin_bit_cast(bf16x8, i < 120 ? xf[i < 120 ? i / 3 : 0] : u32x4{xe0, xe1, 0, 0}), acc[i % 3]);
        SCHED();
        if (n + NB < 246) rd(n + NB);
        if (part == 0 && (i & 1) && (i >> 1) + XW < 40) xf[(i >> 1) + XW] = LD128(vx, hb + ((i >> 1) + XW) * 4096);
        if (n >= 4 && n < 12 && !(n & 1)) stp[(n - 4) >> 1] = LD64(hi * 4096 + r4 * 8, WS_STAT + (unsigned)(g * 8 + ((n - 4) >> 1)) * 1024);
        if (n == 44) {
          float s1 = 0.f, s2 = 0.f;
#pragma unroll
          for (int j = 0; j < 4; ++j) { s1 += __uint_as_float(stp[j][0]); s2 += __uint_as_float(stp[j][1]); }
          s1 = sum32(s1) * (1.f / 640.f); s2 = sum32(s2) * (1.f / 640.f);
          r2 = __builtin_amdgcn_rcpf(fmaxf(s2 - s1 * s1, 0.f) + 1e-5f);
          const unsigned mh = pk2(-s1, 0.f);
          xe0 = hi ? 0u : pk2(-s1, -s1 - bflo(mh));
          xe1 = hi ? 0u : mh;
          OPAQUE(r2); OPAQUE(xe0); OPAQUE(xe1);
        }
        SCHED();
        if (part == 1 && i == 36) { WAITV(0); wset(wdn, 1); SCHED(); }
      });
      STAMP2(54 + part);
      if (part == 1) STAMP(11);
#pragma unroll
      for (int t = 0; t < 3; ++t) {
        unsigned o[4];
#pragma unroll
        for (int d = 0; d < 4; ++d) {
          const f32x2 hv = (f32x2{acc[t][2 * d], acc[t][2 * d + 1]} * r2) * f32x2{acc[t][8 + 2 * d], acc[t][8 + 2 * d + 1]};
          o[d] = pk2(hv[0], hv[1]);
        }
        ST128((u32x4{o[0], o[1], o[2], o[3]}), vx, WS_HH + (unsigned)(g * 192 + 96 * part + 6 * c + 2 * t) * 2048);
      }
      SCHED();
    });
    if (P4H) wset(wc, 18);
    STAMP(12);
  }

  {
    unsigned ln5 = lane16;
    OPAQUE(ln5);
    const int wk = wid & 1;
    const unsigned hb5 = WS_HH + (unsigned)(g * 192) * 2048 + wk * 16384;
    const unsigned v50 = (ln5 >> 8) * 2048 + (64 * (c >> 3) + 16 * wid) * 16 + (ln5 & 255), v51 = v50 ^ 256;
    f32x4 acc[5] = {}, acp[5] = {};
    constexpr int NB = 8;
    bf16x8 wf[NB];
    u32x4 x5[24];
    unsigned so = 0;
    unsigned lv = 0;
    auto rd = [&](int e) {
      if (e % 10 == 0) { wwait2(W_LANDW, lv, 17 + e / 10 + 1); so = ln5 + wk * 10240; OPAQUE(so); so += slot_of(17 + e / 10); OPAQUE(so); }
      wf[e % NB] = ld_b128(sm + so + (e % 10) * 1024);
      if (e % 10 == 3) lv = wraw(W_LANDW);
      if (e % 10 == 9) wset(wc, 17 + e / 10 + 2);
    };
    auto ldx = [&](int part, int j) {
      const int kk = 12 * part + (j >> 1);
      x5[j] = LD128((j & 1) ? v51 : v50, hb5 + (4 * (kk >> 1) + (kk & 1)) * 8192);
    };
    lds_wait_ge(sm0 + L_W + W_RDY, 3);
    STAMP(40);
#pragma unroll
    for (int j = 0; j < XW5I; ++j) ldx(0, j);
#pragma unroll
    for (int e = 0; e < NB; ++e) rd(e);
    SCHED();
    WAITV(XW5I);
    arrive_w(5, WS_F4 + FL);
    STAMP(13);
#pragma unroll
    for (int j = XW5I; j < XW5; ++j) ldx(0, j);
    SCHED();
#pragma unroll
    for (int part = 0; part < 2; ++part) {
#pragma unroll
      for (int i = 0; i < 60; ++i) {
        const int e = 60 * part + i;
        acc[i % 5] = MFMA16(wf[e % NB], __builtin_bit_cast(bf16x8, x5[2 * (i / 5)]), acc[i % 5]);
        SCHED();
        acp[i % 5] = MFMA16(wf[e % NB], __builtin_bit_cast(bf16x8, x5[2 * (i / 5) + 1]), acp[i % 5]);
        SCHED();
        if (e + NB < 120) rd(e + NB);
        if ((part ? XW5B : XW5) + i < 24) ldx(part, (part ? XW5B : XW5) + i);
        SCHED();
      }
      if (part == 0) {
        lds_wait_ge(sm0 + L_W + W_RDY, 4);
        STAMP(41);
#pragma unroll
        for (int j = 0; j < XW5B; ++j) ldx(1, j);
        SCHED();
      }
    }
    for (int sp = 0; wget4(W_CONSW) < 24 && sp < SPIN_CAP; ++sp) {}
    FENCE();
#pragma unroll
    for (int tt = 0; tt < 5; ++tt) *(LAS f32x4*)(sm + slot_of(22) + (wid ^ 1) * 5120 + tt * 1024 + ln5) = acp[tt];
    wset(wdn, 9);
    lds_wait_ge(sm0 + L_W + W_DONE + 4 * (wid ^ 1), 9);
#pragma unroll
    for (int tt = 0; tt < 5; ++tt) acc[tt] += *(LAS f32x4*)(sm + slot_of(22) + wid * 5120 + tt * 1024 + ln5);
    STAMP(14);
    unsigned opk[10];
    const u32x4 h0 = *(LAS u32x4*)(sm + L_OST + wid * 2560 + ln5), h1 = *(LAS u32x4*)(sm + L_OST + wid * 2560 + 1024 + ln5);
    const u32x2 h2 = *(LAS u32x2*)(sm + L_OST + wid * 2560 + 2048 + (ln5 >> 1));
#pragma unroll
    for (int d = 0; d < 10; ++d) {
      const unsigned hv = d < 4 ? h0[d & 3] : d < 8 ? h1[d & 3] : h2[d & 1];
      const unsigned a = pk2(acc[d >> 1][2 * (d & 1)], acc[d >> 1][2 * (d & 1) + 1]);
      opk[d] = pk2(bflo(a) + bflo(hv), bfhi(a) + bfhi(hv));
    }
    const unsigned G5 = ln5 >> 8;
    char* op = out + (size_t)(128 * g + 64 * (c >> 3) + 16 * wid) * 1280 + (c & 7) * 160 + (ln5 & 255) * 80;
    *(u32x4*)(op + G5 * 16) = u32x4{opk[0], opk[1], opk[2], opk[3]};
    *(u32x4*)(op + 64 + G5 * 16) = u32x4{opk[4], opk[5], opk[6], opk[7]};
    *(u32x2*)(op + 128 + G5 * 8) = u32x2{opk[8], opk[9]};
    STAMP(15);
    STAMP_DUMP();
  }
}

extern "C" void kernel_launch(void* const* d_in, const int* in_sizes, int n_in, void* d_out, int out_size, void* d_ws, size_t ws_size,
                              hipStream_t stream) {
  fused_block<<<256, 512, 0, stream>>>((const char*)d_in[0], (const char*)d_in[1], (const char*)d_in[2], (const char*)d_in[4],
                                       (const char*)d_in[5], (const char*)d_in[6], (char*)d_out, (char*)d_ws);
}
```

```cpp
#include <hip/hip_runtime.h>
#include <stdint.h>

typedef unsigned long long u64;
typedef __attribute__((ext_vector_type(8))) short bf16x8;
typedef __attribute__((ext_vector_type(4))) short s16x4;
typedef __attribute__((ext_vector_type(2))) __bf16 bf16x2;
typedef __attribute__((ext_vector_type(16))) float f32x16;
typedef __attribute__((ext_vector_type(4))) float f32x4;
typedef __attribute__((ext_vector_type(2))) float f32x2;
typedef __attribute__((ext_vector_type(4))) unsigned u32x4;
typedef __attribute__((ext_vector_type(2))) unsigned u32x2;
#define LAS __attribute__((address_space(3)))
#define DI __device__ __forceinline__

constexpr unsigned WS_EPOCH = 0, WS_HINT = 1024;
constexpr unsigned FL = 256 * 128;
constexpr unsigned WS_F0 = 4096, WS_F1 = WS_F0 + FL, WS_F2 = WS_F1 + FL, WS_F3 = WS_F2 + FL, WS_F4 = WS_F3 + FL;
constexpr unsigned WS_F1P = WS_F4 + 3 * FL;
constexpr unsigned WS_CSUM = WS_F1P + 2048;
constexpr unsigned WS_ST1 = WS_CSUM + 27 * 12288;
constexpr unsigned WS_CS1 = WS_ST1 + 2048 * 16;
constexpr unsigned WS_STAT = WS_CS1 + 16 * 16 * 512;
constexpr unsigned WS_QKV = 1u << 20;
constexpr unsigned WS_O = WS_QKV + 16 * 240 * 2048;
constexpr unsigned WS_H = WS_O + 16 * 80 * 2048;
constexpr unsigned WS_HH = WS_H + 16 * 80 * 2048;
constexpr unsigned WS_W3 = WS_HH + 16 * 192 * 2048;
constexpr unsigned WS_W4 = WS_W3 + 8 * 100 * 1024;
constexpr unsigned WS_W5 = WS_W4 + 16 * 240 * 1024;
constexpr unsigned WS_END = WS_W5 + 8 * 240 * 1024;
constexpr unsigned WS_DIAG = 32u << 20;
static_assert(WS_STAT + 16 * 8 * 1024 <= WS_QKV && WS_END <= WS_DIAG, "workspace map");

constexpr unsigned L_STAT = 0, L_CS = 1024;
constexpr unsigned L_W = 1536;
constexpr unsigned L_ONES = 1792;
constexpr unsigned L_KT = 2304;
constexpr unsigned L_RP = 2560;
constexpr unsigned L_OST = 2560;
constexpr unsigned L_JUNK = 31488;
constexpr unsigned L_HSTG = 81920;
constexpr unsigned L_WX = 12800;
constexpr int CODE_LINES = 48;
constexpr unsigned L_P1 = 32768;
constexpr unsigned L_K = 12800, L_V = L_K + 5 * 6144, VSTR = 6208;
constexpr unsigned L_RING = 20480, RSLOT = 20480;
constexpr unsigned slot_of(int i) { return L_RING + ((i + 3) % 7) * RSLOT; }
static_assert(L_V + 5 * VSTR <= L_RING + 3 * RSLOT, "LDS map");
enum { W_LAND1 = 0, W_LANDW = 8, W_CONS1 = 16, W_CONSW = 32, W_GO = 48, W_S1DONE = 52, W_STATS = 56, W_WREADY = 60, W_RDY = 64, W_DONE = 80, W_CNT = 96, W_INIT = 128, W_EPO = 176, W_LL = 184 };

#define P4REL 0
#define APRIO 2
#define PDLY 24
#define PSLP 1
#define GDLY 16
#define FDLY 1
#define XW3 8
#define LDLY 16
#define FPUB 36
#define FX2 128
#define ABL 0
#define AMAP 1
#define XW5 10
#define P3REL 3
#define EV 7
#define LP 0
#define NTS " nt"
#define NT1 ""
#define NT2 0
#define NT3 0
#define NT5 0
#define NT6 0
#define NT4 0
#define XW5B 10
#define XW5I 10
#define XW4 10
#define RS 0
#define R1C 0
#define R1G 0
#define LP1 0
#define LP2 0
#define LA 2
#define PB 0
#define LSL 0
#define P3LATE 1
#define TCS 0
#define P4H 1
#define WJ 8
#define RE 0
#define FLW 16
#define X3S 3
#define WIW 1
#define AJ 6
#define OT 0
#define OWB 0
constexpr float QSCALE = 0.22360679774997896f * 1.4426950408889634f;

#define MFMA32(a, b, c) __builtin_amdgcn_mfma_f32_32x32x16_bf16((a), (b), (c), 0, 0, 0)
#define MFMA16(a, b, c) __builtin_amdgcn_mfma_f32_16x16x32_bf16((a), (b), (c), 0, 0, 0)
#define BAR() asm volatile("s_waitcnt lgkmcnt(0)\n\ts_barrier" ::: "memory")
#define SCHED() __builtin_amdgcn_sched_barrier(0)
#define RAWBAR() do { SCHED(); asm volatile("s_barrier" ::: "memory"); SCHED(); } while (0)
#define OPAQUE(v) asm volatile("" : "+v"(v))
#define SGB(m, n) __builtin_amdgcn_sched_group_barrier((m), (n), 0)
#define WAITV(n) asm volatile("s_waitcnt vmcnt(%0)" ::"i"(n) : "memory")

#define STAMP(i) do {} while (0)
#define STAMP2(i) do {} while (0)
#define STAMP_DUMP() do {} while (0)

DI void glds(unsigned voff, const char* sbase, unsigned ldst) {
  unsigned sv;
  asm volatile("s_mov_b32 %0, m0\n\ts_mov_b32 m0, %3\n\ts_nop 2\n\tglobal_load_lds_dwordx4 %1, %2" NTS "\n\ts_mov_b32 m0, %0"
               : "=&s"(sv) : "v"(voff), "s"(sbase), "s"(ldst) : "memory");
}
DI void glds_sc1(unsigned voff, const char* sbase, unsigned ldst) {
  unsigned sv;
  asm volatile("s_mov_b32 %0, m0\n\ts_mov_b32 m0, %3\n\ts_nop 2\n\tglobal_load_lds_dwordx4 %1, %2 sc1\n\ts_mov_b32 m0, %0"
               : "=&s"(sv) : "v"(voff), "s"(sbase), "s"(ldst) : "memory");
}
DI void glds5_sc1(unsigned voff, const char* sbase, unsigned ldst) {
  unsigned sv;
  asm volatile("s_mov_b32 %0, m0\n\ts_mov_b32 m0, %3\n\ts_nop 2\n\t"
               "global_load_lds_dwordx4 %1, %2 offset:-2048 sc1\n\tglobal_load_lds_dwordx4 %1, %2 offset:-1024 sc1\n\t"
               "global_load_lds_dwordx4 %1, %2 sc1\n\tglobal_load_lds_dwordx4 %1, %2 offset:1024 sc1\n\t"
               "global_load_lds_dwordx4 %1, %2 offset:2048 sc1\n\ts_mov_b32 m0, %0"
               : "=&s"(sv) : "v"(voff), "s"(sbase + 2048), "s"(ldst + 2048) : "memory");
}
DI void glds5(unsigned voff, const char* sbase, unsigned ldst) {
  unsigned sv;
  asm volatile("s_mov_b32 %0, m0\n\ts_mov_b32 m0, %3\n\ts_nop 2\n\t"
               "global_load_lds_dwordx4 %1, %2 offset:-2048" NTS "\n\tglobal_load_lds_dwordx4 %1, %2 offset:-1024" NTS "\n\t"
               "global_load_lds_dwordx4 %1, %2" NTS "\n\tglobal_load_lds_dwordx4 %1, %2 offset:1024" NTS "\n\t"
               "global_load_lds_dwordx4 %1, %2 offset:2048" NTS "\n\ts_mov_b32 m0, %0"
               : "=&s"(sv) : "v"(voff), "s"(sbase + 2048), "s"(ldst + 2048) : "memory");
}
DI void glds8(const unsigned (&v)[8], const char* sbase, unsigned ldst) {
  unsigned sv;
  asm volatile("s_mov_b32 %0, m0\n\ts_mov_b32 m0, %10\n\ts_nop 2\n\t"
               "global_load_lds_dwordx4 %1, %9 offset:-4096" NT1 "\n\tglobal_load_lds_dwordx4 %2, %9 offset:-3072" NT1 "\n\t"
               "global_load_lds_dwordx4 %3, %9 offset:-2048" NT1 "\n\tglobal_load_lds_dwordx4 %4, %9 offset:-1024" NT1 "\n\t"
               "global_load_lds_dwordx4 %5, %9" NT1 "\n\tglobal_load_lds_dwordx4 %6, %9 offset:1024" NT1 "\n\t"
               "global_load_lds_dwordx4 %7, %9 offset:2048" NT1 "\n\tglobal_load_lds_dwordx4 %8, %9 offset:3072" NT1 "\n\ts_mov_b32 m0, %0"
               : "=&s"(sv) : "v"(v[0]), "v"(v[1]), "v"(v[2]), "v"(v[3]), "v"(v[4]), "v"(v[5]), "v"(v[6]), "v"(v[7]),
                 "s"(sbase), "s"(ldst + 4096) : "memory");
}
DI void glds10_sc1(unsigned voff, const char* sbase, unsigned ldst) {
  unsigned sv;
  asm volatile("s_mov_b32 %0, m0\n\ts_mov_b32 m0, %4\n\ts_nop 2\n\t"
               "global_load_lds_dwordx4 %1, %3 offset:-4096 sc1\n\tglobal_load_lds_dwordx4 %1, %3 offset:-3072 sc1\n\t"
               "global_load_lds_dwordx4 %1, %3 offset:-2048 sc1\n\tglobal_load_lds_dwordx4 %1, %3 offset:-1024 sc1\n\t"
               "global_load_lds_dwordx4 %1, %3 sc1\n\tglobal_load_lds_dwordx4 %1, %3 offset:1024 sc1\n\t"
               "global_load_lds_dwordx4 %1, %3 offset:2048 sc1\n\tglobal_load_lds_dwordx4 %1, %3 offset:3072 sc1\n\t"
               "s_add_u32 m0, m0, 0x1000\n\ts_nop 0\n\t"
               "global_load_lds_dwordx4 %2, %3 sc1\n\tglobal_load_lds_dwordx4 %2, %3 offset:1024 sc1\n\ts_mov_b32 m0, %0"
               : "=&s"(sv) : "v"(voff), "v"(voff + 4096), "s"(sbase + 4096), "s"(ldst + 4096) : "memory", "scc");
}
DI void glds2_sc1(unsigned voff, const char* sbase, unsigned ldst) {
  unsigned sv;
  asm volatile("s_mov_b32 %0, m0\n\ts_mov_b32 m0, %3\n\ts_nop 2\n\t"
               "global_load_lds_dwordx4 %1, %2 sc1\n\tglobal_load_lds_dwordx4 %1, %2 offset:1024 sc1\n\ts_mov_b32 m0, %0"
               : "=&s"(sv) : "v"(voff), "s"(sbase), "s"(ldst) : "memory");
}
#define ST128(v, voff, soff) __builtin_amdgcn_raw_buffer_store_b128((v), rs, (unsigned)(voff) + (unsigned)(soff), 0, 16 | NT4)
#define ST128R(v, voff, soff) __builtin_amdgcn_raw_buffer_store_b128((v), rs, (unsigned)(voff) + (unsigned)(soff), 0, 16 | NT3)
#define ST64(v, voff, soff) __builtin_amdgcn_raw_buffer_store_b64((v), rs, (unsigned)(voff) + (unsigned)(soff), 0, 16)
#define LD128(voff, soff) __builtin_amdgcn_raw_buffer_load_b128(rs, (unsigned)(voff), (unsigned)(soff), 16)
#define LD64(voff, soff) __builtin_amdgcn_raw_buffer_load_b64(rs, (unsigned)(voff), (unsigned)(soff), 16)
DI bf16x8 ld_b128(LAS char* p) { return *(LAS bf16x8*)p; }
DI s16x4 ld_tr(LAS char* p) { return __builtin_amdgcn_ds_read_tr16_b64_v4i16((LAS s16x4*)p); }
DI bf16x8 ld_tr2(LAS char* p0, LAS char* p1) {
  s16x4 a = ld_tr(p0), b = ld_tr(p1);
  return __builtin_shufflevector(a, b, 0, 1, 2, 3, 4, 5, 6, 7);
}
DI unsigned pk2(float lo, float hi) {
  f32x2 v = {lo, hi};
  return __builtin_bit_cast(unsigned, __builtin_convertvector(v, bf16x2));
}
DI float bflo(unsigned w) { return __uint_as_float(w << 16); }
DI float bfhi(unsigned w) { return __uint_as_float(w & 0xffff0000u); }
DI float sum32(float v) {
  auto r = __builtin_amdgcn_permlane32_swap(__float_as_uint(v), __float_as_uint(v), false, false);
  return __uint_as_float(r[0]) + __uint_as_float(r[1]);
}
DI float sum16(float v) {
  auto r = __builtin_amdgcn_permlane16_swap(__float_as_uint(v), __float_as_uint(v), false, false);
  return __uint_as_float(r[0]) + __uint_as_float(r[1]);
}
DI float sum8(float v) {
  v += __uint_as_float(__builtin_amdgcn_update_dpp(0, __float_as_uint(v), 0xB1, 0xf, 0xf, true));
  v += __uint_as_float(__builtin_amdgcn_update_dpp(0, __float_as_uint(v), 0x4E, 0xf, 0xf, true));
  v += __uint_as_float(__builtin_amdgcn_update_dpp(0, __float_as_uint(v), 0x141, 0xf, 0xf, true));
  return v;
}
DI float max32(float v) {
  auto r = __builtin_amdgcn_permlane32_swap(__float_as_uint(v), __float_as_uint(v), false, false);
  return fmaxf(__uint_as_float(r[0]), __uint_as_float(r[1]));
}
DI u64 mix64(u64 z) {
  z += 0x9E3779B97F4A7C15ull; z = (z ^ (z >> 30)) * 0xBF58476D1CE4E5B9ull; z = (z ^ (z >> 27)) * 0x94D049BB133111EBull;
  return z ^ (z >> 31);
}
DI u64 ld_flag(const char* p) { return __hip_atomic_load((const u64*)p, __ATOMIC_RELAXED, __HIP_MEMORY_SCOPE_AGENT); }
DI void st_flag(char* p, u64 v) { __hip_atomic_store((u64*)p, v, __ATOMIC_RELAXED, __HIP_MEMORY_SCOPE_AGENT); }
DI void wait_flags_from(const char* p, u64 v, u64 epoch) {
  for (unsigned spins = 0; spins < (1u << 18); ++spins) {
    if (__all(v == epoch)) break;
    __builtin_amdgcn_s_sleep(PSLP);
    v = ld_flag(p);
  }
  asm volatile("" ::: "memory");
}
DI void wait_flags(const char* p, u64 epoch) { wait_flags_from(p, ld_flag(p), epoch); }
DI f32x16 mfma32_c(bf16x8 a, bf16x8 b, const f32x16& c) {
  f32x16 d;
  asm volatile("s_nop 1\n\tv_mfma_f32_32x32x16_bf16 %0, %1, %2, %3" : "=&v"(d) : "v"(a), "v"(b), "v"(c));
  return d;
}

DI float dot2(unsigned a, unsigned b, float c) {
  return __builtin_amdgcn_fdot2_f32_bf16(__builtin_bit_cast(bf16x2, a), __builtin_bit_cast(bf16x2, b), c, false);
}
DI int rfl(int v) { return __builtin_amdgcn_readfirstlane(v); }
DI int mini(int a, int b) { return a < b ? a : b; }
typedef __attribute__((ext_vector_type(2))) int i32x2;
typedef __attribute__((ext_vector_type(4))) int i32x4;
template <int I> struct IC { static constexpr int value = I; };
template <int I, int N, class F> DI void static_for(F&& f) { if constexpr (I < N) { f(IC<I>{}); static_for<I + 1, N>(f); } }
#define FENCE() asm volatile("" ::: "memory")
constexpr int SPIN_CAP = 1 << 22;
DI void lds_wait_eq(unsigned v, unsigned addr, unsigned want) {
  unsigned s0, cnt;
  asm volatile("s_mov_b32 %2, 0x400000\n"
               "1:\n\tv_readfirstlane_b32 %1, %0\n\ts_cmp_eq_u32 %1, %4\n\ts_cbranch_scc1 2f\n\t"
               "ds_read_b32 %0, %3\n\ts_sub_u32 %2, %2, 1\n\ts_waitcnt lgkmcnt(0)\n\ts_cbranch_scc0 1b\n"
               "2:"
               : "+v"(v), "=&s"(s0), "=&s"(cnt) : "v"(addr), "s"(want) : "memory", "scc");
}
DI void lds_wait_ge(unsigned addr, unsigned need) {
  unsigned v, s0, cnt;
  asm volatile("s_mov_b32 %2, 0x400000\n"
               "1:\n\tds_read_b32 %0, %3\n\ts_waitcnt lgkmcnt(0)\n\tv_readfirstlane_b32 %1, %0\n\ts_cmp_ge_u32 %1, %4\n\ts_cbranch_scc1 2f\n\t"
               "s_sub_u32 %2, %2, 1\n\ts_cbranch_scc0 1b\n"
               "2:"
               : "=&v"(v), "=&s"(s0), "=&s"(cnt) : "v"(addr), "s"(need) : "memory", "scc");
}
DI void lds_wait_halves(unsigned v, unsigned addr, unsigned need) {
  unsigned s0, s1, cnt;
  asm volatile("s_mov_b32 %3, 0x400000\n"
               "1:\n\tv_readfirstlane_b32 %1, %0\n\ts_lshr_b32 %2, %1, 16\n\ts_and_b32 %1, %1, 0xffff\n\ts_min_u32 %1, %1, %2\n\t"
               "s_cmp_ge_u32 %1, %5\n\ts_cbranch_scc1 2f\n\t"
               "ds_read_b32 %0, %4\n\ts_sub_u32 %3, %3, 1\n\ts_waitcnt lgkmcnt(0)\n\ts_cbranch_scc0 1b\n"
               "2:"
               : "+v"(v), "=&s"(s0), "=&s"(s1), "=&s"(cnt) : "v"(addr), "s"(need) : "memory", "scc");
}

__global__ __launch_bounds__(512, 2) void fused_block(const char* __restrict__ x, const char* __restrict__ wqkv, const char* __restrict__ wo,
                                                      const char* __restrict__ wg, const char* __restrict__ wu, const char* __restrict__ wd,
                                                      char* __restrict__ out, char* ws) {
  __shared__ __attribute__((aligned(16))) char smem_[163840];
  LAS char* sm = (LAS char*)smem_;
  const unsigned sm0 = (unsigned)(uintptr_t)sm;
  const int tid = threadIdx.x, lane = tid & 63;
  const int wid = __builtin_amdgcn_readfirstlane(tid >> 6);
  STAMP(0);
  const int b = blockIdx.x, g = 4 * ((b & 7) >> 1) + (b >> 6), c = 8 * (b & 1) + ((b >> 3) & 7);
  const int wgid = g * 16 + c;
  const int g2 = AMAP == 0 ? g : AMAP == 1 ? (b >> 4) : 8 * (b & 1) + (b >> 5), c2 = AMAP == 0 ? c : AMAP == 1 ? 2 * (b & 7) + ((b >> 3) & 1) : 4 * ((b & 7) >> 1) + ((b >> 3) & 3);
  const int c1 = ((g & 1) && (c & 7) == 7) ? c ^ 8 : c;
  const bool lx = !(wid & 1);
  unsigned vo[8];
  {
    const unsigned rsh = lx ? 3 : 4, rstr = lx ? 1280 : 3840, pstr = (lx ? 8 * 1280 : 4 * 3840) - 1024;
    const unsigned cm = lx ? 7 : 15, csh = lx ? 0 : 2, chx = lx ? 64 : 0;
    const unsigned ch0 = (lane & cm) ^ ((lane >> 4) << csh);
    const unsigned vb = (lane >> rsh) * rstr + 4096;
#pragma unroll
    for (int i = 0; i < 8; ++i) vo[i] = vb + i * pstr + ((ch0 << 4) ^ ((i & 1) ? chx : 0));
  }
  const char* const rsb = (lx ? x : wqkv) + (lx ? g * (128 * 1280) : c1 * 256);
  const unsigned hstep = lx ? 64 * 1280 : 32 * 3840, sstep = lx ? 128 : 64 * 3840, rdst = sm0 + L_P1 + (wid & 1) * 16384;
  const int rot1 = (R1C * (c & 7) + R1G * (g & 3)) % 10;
  if (c1 < 15 && wid < 4) {
#pragma unroll
    for (int s = 0; s < 2; ++s) glds8(vo, rsb + (rot1 + s == 10 ? 0 : rot1 + s) * sstep + (wid >> 1) * hstep, rdst + s * 32768 + (wid >> 1) * 8192);
  }
  SCHED();
  const auto rs = __builtin_amdgcn_make_buffer_rsrc(ws, 0, WS_END, 0x00020000);
  const bool isC = wid < 4, isL = (wid >> 1) == 2;
  const int l31 = lane & 31, hi = lane >> 5, G = lane >> 4, q4 = (lane & 15) >> 2, p4 = lane & 3, l15 = lane & 15;
  STAMP(6);
  u64 eraw = 0, epoch = 0;
  auto load_epoch = [&]() { unsigned z = 0; OPAQUE(z); eraw = ld_flag(ws + WS_EPOCH + z); };
  auto get_epoch = [&]() {
    const u64 e = mix64(eraw);
    const unsigned e0 = __builtin_amdgcn_readfirstlane((unsigned)e), e1 = __builtin_amdgcn_readfirstlane((unsigned)(e >> 32));
    epoch = ((u64)e1 << 32) | e0;
  };
  auto wset = [&](int w, int v) { FENCE(); *(volatile LAS int*)(sm + L_W + w) = v; FENCE(); };
  auto wget = [&](int w) { return rfl(*(volatile LAS int*)(sm + L_W + w)); };
  auto wget4 = [&](int w) { const i32x4 v = *(volatile LAS i32x4*)(sm + L_W + w); return rfl(mini(mini(v[0], v[1]), mini(v[2], v[3]))); };
  auto wwait = [&](int w, int need) { for (int sp = 0; wget(w) < need && sp < SPIN_CAP; ++sp) __builtin_amdgcn_s_sleep(1); FENCE(); };
  auto wraw = [&](int w) { return *(volatile LAS unsigned*)(sm + L_W + w); };
  auto wset16 = [&](int w, int v) { FENCE(); *(volatile LAS short*)(sm + L_W + w) = (short)v; FENCE(); };
  auto wwait2 = [&](int w, unsigned first, int need) { lds_wait_halves(first, sm0 + L_W + w, need); };
  auto count = [&](int k) {
    unsigned old = 0;
    if (lane == 0) old = __hip_atomic_fetch_add((LAS unsigned*)(sm + L_W + W_CNT + 4 * k), 1u, __ATOMIC_RELAXED, __HIP_MEMORY_SCOPE_WORKGROUP);
    return rfl(old);
  };
  auto raise = [&](unsigned off) { if (lane < FLW) st_flag(ws + off + lane * 8, epoch); };
  auto arrive_n = [&](int k, unsigned flag_base, int n) {
    if (count(k) == n - 1) {
      raise(flag_base + (g * 16 + c1) * 128);
      if (k == 1 && c1 == 14) raise(WS_HINT + g * 128);
    }
  };
  if (wid == 7) {
    u64 pc;
    asm volatile("s_getpc_b64 %0" : "=s"(pc));
    unsigned sv;
    const int ln = lane < CODE_LINES ? lane : 0;
    asm volatile("s_mov_b32 %0, m0\n\ts_mov_b32 m0, %3\n\ts_nop 2\n\tglobal_load_lds_dword %1, %2\n\ts_mov_b32 m0, %0"
                 : "=&s"(sv) : "v"((((b >> 3) & 7) * CODE_LINES + ln) * 128), "s"(pc), "s"(sm0 + L_JUNK) : "memory");
  }
  if (wid == 6) *(LAS unsigned*)(sm + L_W + 4 * lane) = 0;
  if (wid == 7) *(LAS u32x2*)(sm + L_ONES + 8 * lane) = u32x2{0x3f803f80u, 0x3f803f80u};
  if (wid == 6 && lane < 12) *(LAS u32x4*)(sm + L_KT + 16 * lane) = u32x4{lane < 8 - 4 * g2 ? 0x3f803f80u : 0x00003f80u, 0, 0, 0};

  auto arrive_w = [&](int k, unsigned flag_base) {
    if (count(k) == 3) raise(flag_base + wgid * 128);
  };
  bf16x8 rf[10];
  unsigned rvo = 0, rvo1 = 0, rstep5 = 0;
  int rn = 0, roct = 0;
  auto wide_read = [&](unsigned st, unsigned dst, unsigned nk5) {
    LAS char* p = sm + st + q4 * 1280 + G * 320;
#pragma unroll
    for (int j = 0; j < 10; ++j) {
      const int tt = j % 5;
      LAS char* pj = p + (j / 5) * 160 + (tt < 4 ? 64 * (tt >> 1) + 16 * p4 + 8 * (tt & 1) : 128 + 8 * p4);
      rf[j] = ld_tr2(pj, pj + 4 * 1280);
    }
    rvo = rvo1 = dst + G * 2 * nk5 * 1024 + l15 * 16; rstep5 = nk5 * 1024; rn = 10;
  };
  auto retile_store = [&](int j) {
    const int o = roct + (j >> 1);
    ST128R(__builtin_bit_cast(u32x4, rf[j]), (j & 1) ? rvo1 : rvo,
          rn == 10 ? (j % 5) * 1024 + (j / 5) * rstep5 : (o >> 1) * 3072 + (o & 1) * 512);
  };
  const int su = wgid * 3 + wid - 5;
  const unsigned rst = L_RP + (wid == 4 ? 0 : 10240 + (wid - 5) * 6144);
  auto late_load = [&](int w) {
    const unsigned rsw = sm0 + L_RP + (w == 4 ? 0 : 10240 + (w - 5) * 6144);
    const int suw = wgid * 3 + w - 5;
    if (w == 4) {
      if (wgid < 192) {
        glds5(lane * 16, wd + (size_t)wgid * 10240, rsw);
        glds5(lane * 16, wd + (size_t)wgid * 10240 + 5120, rsw + 5120);
      }
    } else if (suw < 648) {
      const int cl = suw % 24, rgp = suw / 24;
      const unsigned vr = LP == 0 ? (lane >> 3) * 3072 + (lane & 7) * 16 : LP == 1 ? lane * 16 : (lane >> 5) * 3072 + (lane & 31) * 16;
#pragma unroll
      for (int i = 0; i < 3; ++i) {
        const size_t so = LP == 0 ? (size_t)mini(3 * rgp + i, 79) * 8 * 3072 + cl * 128 : LP == 1 ? (size_t)mini(suw, 639) * 3072 + i * 1024
                          : (size_t)(mini(wgid / 6, 34) * 18 + (w - 5) * 6 + 2 * i) * 3072 + (wgid % 6) * 512;
        glds(vr, wg + so, rsw + i * 1024);
        glds(vr, wu + so, rsw + 3072 + i * 1024);
      }
    }
  };
  auto late_read = [&]() {
    WAITV(0);
    if (wid == 4) {
      const int pos = 96 * ((wgid % 12) / 6) + 6 * (wgid / 12) + wgid % 6;
      if (wgid < 192) wide_read(rst, WS_W5 + (pos >> 2) * 5120 + (pos & 3) * 256, 240);
    } else if (su < 648) {
      const int cl = su % 24, rgp = su / 24;
      float cs[2] = {0.f, 0.f};
      rn = rgp == 26 ? 4 : 6;
#pragma unroll
      for (int j = 0; j < 6; ++j) {
        LAS char* p = sm + rst + (G & 1) * 3072 + ((j >> 1) * 8 + q4) * 128 + (2 * (j & 1) + hi) * 32 + 16 * (p4 & 1) + 8 * (p4 >> 1);
        rf[j] = ld_tr2(p, p + 4 * 128);
        const u32x4 w = __builtin_bit_cast(u32x4, rf[j]);
        const unsigned one = j < rn ? 0x3f803f80u : 0u;
#pragma unroll
        for (int d = 0; d < 4; ++d) cs[j & 1] = dot2(w[d], one, cs[j & 1]);
      }
      {
        const unsigned t0 = 4 * cl + hi, t1 = t0 + 2;
        rvo = WS_W4 + ((t0 / 3) * 120 + t0 % 3) * 1024 + l31 * 16;
        rvo1 = WS_W4 + ((t1 / 3) * 120 + t1 % 3) * 1024 + l31 * 16;
      }
      roct = 3 * rgp;
      const unsigned cvo = lane * 4;
#pragma unroll
      for (int jp = 0; jp < 2; ++jp)
        __builtin_amdgcn_raw_buffer_store_b32(__float_as_uint(cs[jp]), rs, cvo + WS_CSUM + (unsigned)(rgp * 12288 + cl * 512 + jp * 256), 0, 16);
    }
    FENCE();
    if (lane == 0) __hip_atomic_fetch_add((LAS unsigned*)(sm + L_W + W_S1DONE), 1u, __ATOMIC_RELAXED, __HIP_MEMORY_SCOPE_WORKGROUP);
    FENCE();
  };
  if (isL && c1 < 15) {
    unsigned vx[8], vw[8];
#pragma unroll
    for (int i = 0; i < 8; ++i) {
      vx[i] = (lane >> 3) * 1280 + 4096 + i * (8 * 1280 - 1024) + ((((lane & 7) ^ (lane >> 4)) << 4) ^ ((i & 1) * 64));
      vw[i] = (lane >> 4) * 3840 + 4096 + i * (4 * 3840 - 1024) + (((lane & 15) ^ ((lane >> 4) << 2)) << 4);
    }
    int s = 2 + (wid & 1);
    BAR();
    if (LP1) __builtin_amdgcn_s_setprio(LP1);
#pragma unroll 1
    for (; s < 10; s += 2) {
      const int kb = rot1 + s >= 10 ? rot1 + s - 10 : rot1 + s;
      const char* px = x + g * (128 * 1280) + kb * 128;
      const char* pw = wqkv + c1 * 256 + kb * (64 * 3840);
      if (PB && s >= PB) { WAITV(0); wset(W_INIT + 4 * (s - 2), 0x01010101); }
      for (int sp = 0; wget4(W_CONS1) < s - 3 && sp < SPIN_CAP; ++sp) { if (LSL) __builtin_amdgcn_s_sleep(1); }
      STAMP2(7 + s / 2);
      FENCE();
      const unsigned d = sm0 + L_P1 + (s & 3) * 32768;
      glds8(vx, px, d);
      glds8(vw, pw, d + 16384);
      WAITV(16);
      if (s >= 4) wset(W_INIT + 4 * (s - 2), 0x01010101);
      STAMP(16 + s - 2);
      glds8(vx, px + 64 * 1280, d + 8192);
      glds8(vw, pw + 32 * 3840, d + 24576);
    }
    WAITV(0);
    wset(W_INIT + 4 * (s - 2), 0x01010101);
    STAMP(24);
    if (LP1) __builtin_amdgcn_s_setprio(0);
    load_epoch();
    if (wid == 4 ? !(EV & 4) : !(EV & 2)) late_load(wid);
    get_epoch();
    if (wid == 4 && (EV & 4)) wwait(W_STATS, 1);
    if (wid == 5 && (EV & 2)) wwait(W_LL, 1);
    late_read();
  } else if (isC && c1 < 15) {
    const int wn = wid & 1, wt = wid >> 1;
    f32x16 acc[2][2] = {};
    const unsigned ax = L_P1 + (64 * wt + l31) * 128 + ((hi ^ ((l31 >> 1) & 7)) << 4);
    const unsigned aw = L_P1 + 16384 + (8 * hi + q4) * 256 + (((8 * wn + 2 * (G & 1) + (p4 & 1)) ^ (q4 << 2)) << 4) + 8 * (p4 >> 1);
    bf16x8 xf[4][2], wf[4][2];
    auto rd = [&](int ks, unsigned so) {
#pragma unroll
      for (int t = 0; t < 2; ++t) {
        wf[ks][t] = ld_tr2(sm + (((aw ^ (t * 64)) + so) + ks * 4096), sm + (((aw ^ (t * 64)) + so) + ks * 4096 + 1024));
        xf[ks][t] = ld_b128(sm + (((ax ^ (ks * 32)) + so) + t * 4096));
      }
    };
    auto wset8 = [&](int w) { FENCE(); *(volatile LAS char*)(sm + L_W + w) = 1; FENCE(); };
    BAR();
    WAITV(8);
    wset8(W_INIT + wid);
    lds_wait_eq(0u, sm0 + L_W + W_INIT, 0x01010101u);
    STAMP(16);
    rd(0, 0); rd(1, 0);
    if (LA == 3) rd(2, 0);
    unsigned lv = 0;
    SCHED();
#pragma unroll 1
    for (int s = 0; s < 10; ++s) {
      const unsigned so = (s & 3) * 32768, sn = ((s + 1) & 3) * 32768;
#pragma unroll
      for (int ks = 0; ks < 4; ++ks) {
        const int wn1 = W_INIT + 4 * (s < 9 ? s + 1 : 9);
        if (LA == 2 && ks == 0) lv = wraw(wn1);
        if (LA == 3 && ks == 3) lv = wraw(W_INIT + 4 * (s < 8 ? s + 2 : 9));
        if (ks == 4 - LA) {
          if (s == 0) { WAITV(0); wset8(wn1 + wid); }
          lds_wait_eq(lv, sm0 + L_W + wn1, 0x01010101u);
          STAMP2(17 + s);
          SCHED();
        }
        if (ks < 4 - LA) rd(ks + LA, so); else rd(ks + LA - 4, sn);
        if (ks == 3 - LA) wset(W_CONS1 + 4 * wid, s + 1);
#pragma unroll
        for (int tj = 0; tj < 2; ++tj)
#pragma unroll
          for (int ti = 0; ti < 2; ++ti) acc[tj][ti] = MFMA32(wf[ks][tj], xf[ks][ti], acc[tj][ti]);
        SGB(0x8, 1); SGB(0x100, 2); SGB(0x8, 1); SGB(0x100, 2); SGB(0x8, 1); SGB(0x100, 2); SGB(0x8, 1);
        SCHED();
      }
    }
    STAMP(1);
    wwait(W_STATS, 1);
    {
      const u32x2 e = *(volatile LAS u32x2*)(sm + L_W + W_EPO);
      epoch = ((u64)(unsigned)rfl(e[1]) << 32) | (unsigned)rfl(e[0]);
    }
    const float sc = (c1 < 5) ? QSCALE : 1.f;
#pragma unroll
    for (int ti = 0; ti < 2; ++ti) {
      const int tok = 64 * wt + 32 * ti + l31;
      const f32x2 st = *(LAS f32x2*)(sm + L_STAT + tok * 8);
      const float r = st[1] * sc, nm = -st[0];
#pragma unroll
      for (int tj = 0; tj < 2; ++tj)
#pragma unroll
        for (int hf = 0; hf < 2; ++hf) {
          const int col = 64 * wn + 32 * tj + 16 * hf + 8 * hi;
          const f32x4 ca = *(LAS f32x4*)(sm + L_CS + col * 4), cb = *(LAS f32x4*)(sm + L_CS + col * 4 + 16);
          float v[8];
#pragma unroll
          for (int j = 0; j < 4; ++j) {
            v[j] = (acc[tj][ti][8 * hf + j] + nm * ca[j]) * r;
            v[4 + j] = (acc[tj][ti][8 * hf + 4 + j] + nm * cb[j]) * r;
          }
          u32x4 o = {pk2(v[0], v[1]), pk2(v[2], v[3]), pk2(v[4], v[5]), pk2(v[6], v[7])};
          ST128(o, hi * 2048 + tok * 16, WS_QKV + (unsigned)(g * 240 + 16 * c1 + 8 * wn + 4 * tj + 2 * hf) * 2048);
        }
    }
    WAITV(0);
    arrive_n(1, WS_F1, 4);
  } else {
    const int sw = wid & 1;
    const bool srv = wid >= 6;
    load_epoch();
    u32x4 v[10];
    if (srv) {
      const char* src = sw ? wqkv + (size_t)(40 * g) * 3840 + (c1 < 15 ? c1 : 0) * 256 : x + (size_t)(128 * g + 8 * c1) * 1280;
      const unsigned vs = sw ? G * 3840 + l15 * 16 : (lane >> 3) * 1280 + (lane & 7) * 16, st = sw ? 4 * 3840 : 128;
#pragma unroll
      for (int i = 0; i < 10; ++i) v[i] = NT2 ? __builtin_nontemporal_load((const u32x4*)(src + i * st + vs)) : *(const u32x4*)(src + i * st + vs);
    }
    SCHED();
    BAR();
    if (srv) {
      STAMP(15);
      if (sw == 0) {
        float s = 0.f, q = 0.f;
#pragma unroll
        for (int i = 0; i < 10; ++i)
#pragma unroll
          for (int j = 0; j < 4; ++j) { s = dot2(v[i][j], 0x3f803f80u, s); q = dot2(v[i][j], v[i][j], q); }
        s = sum8(s); q = sum8(q);
        const float mu = s * (1.f / 640.f);
        const float rstd = __builtin_amdgcn_rsqf(fmaxf(q * (1.f / 640.f) - mu * mu, 0.f) + 1e-5f);
        if ((lane & 3) == 0) ST64((u32x2{__float_as_uint(mu), __float_as_uint(rstd)}), (lane & 56) + (lane & 4) * 16, WS_ST1 + (unsigned)(16 * g + c1) * 128);
      } else {
        float a[8] = {};
#pragma unroll
        for (int i = 0; i < 10; ++i)
#pragma unroll
          for (int j = 0; j < 4; ++j) { a[2 * j] += bflo(v[i][j]); a[2 * j + 1] += bfhi(v[i][j]); }
#pragma unroll
        for (int j = 0; j < 8; ++j) a[j] = sum32(sum16(a[j]));
        if (lane < 32) {
          const f32x4 o = (lane & 16) ? f32x4{a[4], a[5], a[6], a[7]} : f32x4{a[0], a[1], a[2], a[3]};
          __builtin_amdgcn_raw_buffer_store_b128(__builtin_bit_cast(u32x4, o), rs, l15 * 32 + (lane & 16) + WS_CS1 + (unsigned)(c1 * 16 + g) * 512, 0, 16 | NT6);
        }
      }
      get_epoch();
      WAITV(0);
      arrive_n(0, WS_F0, 2);
      STAMP(16);
    } else {
      get_epoch();
      if (wid < 5) {
        const int o = 5 * g + wid;
        const unsigned stg = L_HSTG + wid * 10240;
        glds5(lane * 16, wo + (size_t)o * 10240, sm0 + stg);
        glds5(lane * 16, wo + (size_t)o * 10240 + 5120, sm0 + stg + 5120);
        WAITV(0);
        const int pos = wid == 2 ? 64 + g : 4 * g + wid - (wid > 2);
        wide_read(stg, WS_W3 + (pos >> 2) * 5120 + (pos & 3) * 256, 100);
#pragma unroll
        for (int j = 0; j < 10; ++j)
          __builtin_amdgcn_raw_buffer_store_b128(__builtin_bit_cast(u32x4, rf[j]), rs, rvo + (j % 5) * 1024 + (j / 5) * rstep5, 0, 16 | NT5);
        rn = 0;
        WAITV(0);
        arrive_n(1, WS_F1, 5);
      }
    }
    if (wid == 7 && c1 < 15) {
      if (EV & 1) late_load(7);
      if (EV & 4) late_load(4);
      __builtin_amdgcn_s_sleep(FDLY);
      wait_flags(ws + WS_F0 + (lane < 16 ? g * 16 + lane : (l15 * 16 + c1)) * 128, epoch);
      const u32x4 gs = LD128((lane >> 2) * 128 + p4 * 16, WS_ST1 + (unsigned)g * 2048);
      f32x4 t = {};
#pragma unroll
      for (int i = 0; i < 8; ++i) t += __builtin_bit_cast(f32x4, LD128(hi * 4096 + l31 * 16, WS_CS1 + (unsigned)(c1 * 16 + i) * 512));
#pragma unroll
      for (int j = 0; j < 4; ++j) t[j] = sum32(t[j]);
      *(LAS u32x4*)(sm + L_STAT + lane * 16) = gs;
      if (lane < 32) *(LAS f32x4*)(sm + L_CS + lane * 16) = t;
      *(volatile LAS u32x2*)(sm + L_W + W_EPO) = u32x2{(unsigned)epoch, (unsigned)(epoch >> 32)};
      wset(W_STATS, 1);
      STAMP(19);
    }
    if (wid >= 4) {
      if (!((EV & 1) && wid == 7 && c1 < 15)) late_load(wid);
      if ((EV & 2) && wid == 6 && c1 < 15) { late_load(5); WAITV(0); wset(W_LL, 1); }
      late_read();
    }
    STAMP(17);
    if (wid == 6) {
      if (c1 < 15) { for (int sp = 0; wget(W_CNT + 4) < 4 && sp < SPIN_CAP; ++sp) __builtin_amdgcn_s_sleep(1); __builtin_amdgcn_s_sleep(GDLY); }
      else wait_flags(ws + WS_HINT + g * 128, epoch);
      wwait(W_S1DONE, 4);
      const int i = lane < 18 ? lane : 0;
      const int which = i / 6, po = which ? (i % 6) >> 1 : 0, e = i & 1;
      const int tile = (640 * which + 40 * c2 + 39 * e) >> 7;
      const int pn = g2 - po < 0 ? 0 : g2 - po;
      wait_flags(ws + WS_F1 + (pn * 16 + tile) * 128, epoch);
      wset(W_GO, 1);
    }
  }
  const char* wsrc3 = ws + WS_W3 + (c & 7) * 102400 + (wid & 1) * 10240;
  const char* wsrc4 = ws + WS_W4 + c * 245760 + (wid & 1) * 10240 - 5 * 20480;
  const char* wsrc5 = ws + WS_W5 + (c & 7) * 245760 + (wid & 1) * 10240 - 17 * 20480;
  auto wissue_half = [&](int st, int h) {
    const char* sb = (st < 5 ? wsrc3 : st < 17 ? wsrc4 : wsrc5) + st * 20480 + h * 5120;
    glds5_sc1(lane * 16, sb, sm0 + L_RING + ((st + 3) % 7) * RSLOT + (wid & 1) * 10240 + h * 5120);
  };
  STAMP(2);

  bool wready;
  {
    const int hs = wid >> 2, qb = wid & 3;
    if (!isC) {
      const int bk = wid & 3, pn = (bk == 3 || g2 - bk < 0) ? g2 : g2 - bk;
      const char* sA = ws + WS_QKV + (size_t)(pn * 240 + (bk == 3 ? 0 : 80) + 5 * c2) * 2048;
      const unsigned dA = sm0 + (bk == 3 ? L_OST : L_K + (2 - bk) * 2048), dstr = bk == 3 ? 2048 : 6144, dB = sm0 + L_V + (2 - bk) * 2048;
      if (wid == 7) {
        if (c1 == 15) wait_flags(ws + WS_F1 + (l15 * 16 + 15) * 128, epoch);
        if (__all(ld_flag(ws + WS_F1 + (l15 * 16 + 15) * 128) == epoch)) wset(W_WREADY, 1);
      }
      wwait(W_GO, 1);
      STAMP(3);
#pragma unroll
      for (int j = 0; j < 5; ++j) glds2_sc1(lane * 16, sA + j * 2048, dA + j * dstr);
      if (bk < 3) {
#pragma unroll
        for (int j = 0; j < 5; ++j) glds2_sc1(lane * 16, sA + (80 + j) * 2048, dB + j * VSTR);
      }
    }
    const unsigned ak0 = L_K + (3 * hs + hi) * 6144 + l31 * 16, ak1 = L_K + 2 * 6144 + l31 * 16;
    const int kb_lo = (g2 >= 2) ? qb : (g2 == 1 ? 4 : 8), kb_hi = qb + 8;
    const float NINF = -__builtin_inff();
    f32x16 cD, cF;
#pragma unroll
    for (int r = 0; r < 16; ++r) {
      const bool low = (r & 3) + 8 * (r >> 2) + 4 * hi <= l31;
      cD[r] = low ? 0.f : NINF;
      cF[r] = low ? NINF : 0.f;
    }
    const bool isOne = (G & 1) && p4;
    const unsigned av0 = isOne ? L_ONES
                               : L_V + (4 * hi + q4) * 16 + ((G & 1) ? 2 * VSTR + 8 * hs : (3 * hs + (p4 & 1)) * VSTR + 8 * (p4 >> 1));
    const unsigned avs = isOne ? 0 : 512;
    unsigned a0 = ak0 + qb * 512, a1 = hi ? L_KT + qb * 16 : ak1 + qb * 512, av = av0 + qb * avs;
    const unsigned a1s = hi ? 16 : 512;
    asm volatile("" : "+v"(cD), "+v"(cF), "+v"(a0), "+v"(a1), "+v"(av));
    if (wid >= 4 && wid < 7) WAITV(10); else WAITV(0);
    BAR();
    STAMP(4);
    wready = wget(W_WREADY);
    const bool wiss = wready && (wid >> 1) == WIW;
    const int row = 32 * qb + l31;
    u32x4 q0 = *(LAS u32x4*)(sm + L_OST + (3 * hs + hi) * 2048 + row * 16), q1 = *(LAS u32x4*)(sm + L_OST + 2 * 2048 + row * 16);
    if (hi || hs) { q1[0] = 0; q1[1] = 0; }
    if (hi || !hs) { q1[2] = 0; q1[3] = 0; }
    const bf16x8 qf0 = __builtin_bit_cast(bf16x8, q0), qf1 = __builtin_bit_cast(bf16x8, q1);
    auto vmax = [&](const f32x16& s, float m) {
#pragma unroll
      for (int r = 0; r < 16; r += 2) m = fmaxf(fmaxf(m, s[r]), s[r + 1]);
      return m;
    };
    f32x16 o;
    float rl, m;
    auto finish = [&]() {
      auto sw = __builtin_amdgcn_permlane32_swap(__float_as_uint(o[8]), __float_as_uint(o[8]), false, false);
      const float l = __uint_as_float(sw[1]);
      rl = __builtin_amdgcn_rcpf(l);
      float am = 0.f;
#pragma unroll
      for (int r = 0; r < 12; r += 2) am = fmaxf(fmaxf(am, fabsf(o[r])), fabsf(o[r + 1]));
      return (bool)__all(l <= 0x1p30f && l >= 0x1p-30f && am < __builtin_inff());
    };
    {
      f32x16 s[2];
      bf16x8 k0 = ld_b128(sm + a0 + 8 * 512), k1 = ld_b128(sm + a1 + 8 * a1s);
      s[1] = mfma32_c(k0, qf0, cD);
      s[0] = mfma32_c(k1, qf1, s[1]);
      asm volatile("s_nop 11" : "+v"(s[0]));
      m = max32(vmax(s[0], NINF));
      u32x4 q2 = q1;
      if (hi) q2[0] = pk2(-m, -3.0e38f);
      const bf16x8 qf2 = __builtin_bit_cast(bf16x8, q2);
      s[0] = MFMA32(k1, qf2, s[1]);
      k0 = ld_b128(sm + a0); k1 = ld_b128(sm + a1);
      o = f32x16{};
      SCHED();
      if (RS == 1 && rn) { retile_store(0); retile_store(1); WAITV(2); }
      else if (RS == 2 && rn) { retile_store(0); retile_store(1); retile_store(2); retile_store(3); WAITV(4); }
      else WAITV(0);
      BAR();
      SCHED();
#pragma unroll
      for (int j = 0; j < 9; ++j) {
        if (APRIO == 1) { if (((j + hs) & 1)) __builtin_amdgcn_s_setprio(1); else __builtin_amdgcn_s_setprio(0); }
        if (APRIO == 2 && j == 0 && hs) __builtin_amdgcn_s_setprio(1);
        if (APRIO == 2 && j == AJ && hs) __builtin_amdgcn_s_setprio(0);
        f32x16& sc = s[j & 1];
        f32x16& sn = s[(j + 1) & 1];
        unsigned avj = av + (j == 0 ? 8 : j - 1) * avs;
        bf16x8 vf0 = ld_tr2(sm + avj, sm + avj + 128), vf1 = ld_tr2(sm + avj + 256, sm + avj + 384);
        unsigned pw[8];
        if (j < 8) sn = MFMA32(k0, qf0, j == 0 ? cF : f32x16{});
        SCHED();
#pragma unroll
        for (int r = 0; r < 2; ++r) pw[r] = pk2(__builtin_amdgcn_exp2f(sc[2 * r]), __builtin_amdgcn_exp2f(sc[2 * r + 1]));
        SCHED();
        if (j < 8) sn = MFMA32(k1, qf2, sn);
        if (j < 7) { a1 += a1s; k0 = ld_b128(sm + a0 + (j + 1) * 512); k1 = ld_b128(sm + a1); }
        SCHED();
#pragma unroll
        for (int r = 2; r < 5; ++r) pw[r] = pk2(__builtin_amdgcn_exp2f(sc[2 * r]), __builtin_amdgcn_exp2f(sc[2 * r + 1]));
        SCHED();
        o = MFMA32(vf0, __builtin_bit_cast(bf16x8, (u32x4{pw[0], pw[1], pw[2], pw[3]})), o);
        SCHED();
#pragma unroll
        for (int r = 5; r < 8; ++r) pw[r] = pk2(__builtin_amdgcn_exp2f(sc[2 * r]), __builtin_amdgcn_exp2f(sc[2 * r + 1]));
        SCHED();
        o = MFMA32(vf1, __builtin_bit_cast(bf16x8, (u32x4{pw[4], pw[5], pw[6], pw[7]})), o);
        SCHED();
        if (RS == 0 && j + RE < 5 && 2 * (j + RE) < rn) { retile_store(2 * (j + RE)); retile_store(2 * (j + RE) + 1); }
        if (RS == 1 && j < 2 && 4 * j + 2 < rn) { retile_store(4 * j + 2); retile_store(4 * j + 3); }
        if (RS == 1 && j < 2 && 4 * j + 4 < rn) { retile_store(4 * j + 4); retile_store(4 * j + 5); }
        if (RS == 2 && j < 3 && 2 * j + 4 < rn) { retile_store(2 * j + 4); retile_store(2 * j + 5); }
        if (OT && j == 0 && wid == 0) {
          unsigned sv;
          asm volatile("s_mov_b32 %0, m0\n\ts_mov_b32 m0, %3\n\ts_nop 2\n\tglobal_load_lds_dword %1, %2\n\tglobal_load_lds_dword %1, %2 offset:128\n\ts_mov_b32 m0, %0"
                       : "=&s"(sv) : "v"(lane * 1280), "s"(out + (size_t)(128 * g + 64 * (c >> 3)) * 1280 + (c & 7) * 160), "s"(sm0 + L_V + 5 * VSTR) : "memory");
        }
        if (wiss) {
#pragma unroll
          for (int h = 0; h < 8; ++h)
            if (h * WJ / 8 == j) wissue_half(h >> 1, h & 1);
        }
      }
    }
    if (APRIO) __builtin_amdgcn_s_setprio(0);
    if (!finish()) {
      int lq = l31 - 4 * hi;
      OPAQUE(lq);
#pragma unroll 1
      for (int k2 = kb_lo; k2 < kb_hi; ++k2) {
        f32x16 cz;
#pragma unroll
        for (int r = 0; r < 16; ++r) cz[r] = (k2 == qb && (r & 3) + 8 * (r >> 2) <= lq) ? NINF : 0.f;
        f32x16 s = MFMA32(ld_b128(sm + ak0 + k2 * 512), qf0, cz);
        s = MFMA32(ld_b128(sm + ak1 + k2 * 512), qf1, s);
        m = vmax(s, m);
      }
      m = max32(m);
      o = f32x16{};
#pragma unroll 1
      for (int kb = kb_lo; kb <= kb_hi; ++kb) {
        f32x16 cin;
#pragma unroll
        for (int r = 0; r < 16; ++r) cin[r] = ((r & 3) + 8 * (r >> 2) <= lq ? kb == qb : kb == kb_hi) ? NINF : -m;
        f32x16 s = MFMA32(ld_b128(sm + ak0 + kb * 512), qf0, cin);
        s = MFMA32(ld_b128(sm + ak1 + kb * 512), qf1, s);
        const unsigned av = av0 + kb * avs;
        bf16x8 vf0 = ld_tr2(sm + av, sm + av + 128), vf1 = ld_tr2(sm + av + 256, sm + av + 384);
        unsigned pw[8];
#pragma unroll
        for (int r = 0; r < 8; ++r) pw[r] = pk2(__builtin_amdgcn_exp2f(s[2 * r]), __builtin_amdgcn_exp2f(s[2 * r + 1]));
        o = MFMA32(vf0, __builtin_bit_cast(bf16x8, (u32x4{pw[0], pw[1], pw[2], pw[3]})), o);
        o = MFMA32(vf1, __builtin_bit_cast(bf16x8, (u32x4{pw[4], pw[5], pw[6], pw[7]})), o);
      }
      (void)finish();
    }
    const unsigned ob = WS_O + (unsigned)(g2 * 80) * 2048;
    ST128((u32x4{pk2(o[0] * rl, o[1] * rl), pk2(o[2] * rl, o[3] * rl), pk2(o[4] * rl, o[5] * rl), pk2(o[6] * rl, o[7] * rl)}),
          hi * 2048 + row * 16, ob + (4 * c2 + 2 * hs) * 2048);
    if (!hi) ST64((u32x2{pk2(o[8] * rl, o[9] * rl), pk2(o[10] * rl, o[11] * rl)}), row * 8, ob + (64 + c2) * 2048 + hs * 1024);
    STAMP(5);
    WAITV(0);
    if (count(3) == 7) {
      raise(WS_F2 + (g2 * 16 + c2) * 128);
      if (lane == 0) st_flag(ws + WS_F1P + wgid * 8, epoch);
    }
    STAMP(6);
    if (isC) wwait(W_CNT + 12, 8);
  }

  if (isL) {
    int issued = 4;
    if (!wready) {
#pragma unroll 1
      for (int i = 0; i < 1; ++i) wait_flags(ws + WS_F1 + (l15 * 16 + 15) * 128, epoch);
      issued = 0;
    }
    const int wl = W_LANDW + 2 * (wid & 1);
    wwait(W_CNT + 12, 8);
    if (wready) wset16(wl, 4);
    for (; issued < 5; ++issued) { const int st = rfl(issued); wissue_half(st, 0); wissue_half(st, 1); }
    WAITV(0);
    wset16(wl, 5);
    __builtin_amdgcn_s_sleep(LDLY);
    {
      u64 f[4];
#pragma unroll
      for (int i = 0; i < 4; ++i) f[i] = ld_flag(ws + WS_F1P + i * 512 + lane * 8);
#pragma unroll
      for (int i = 0; i < 4; ++i) wait_flags_from(ws + WS_F1P + i * 512 + lane * 8, f[i], epoch);
    }
    STAMP(30);
    if (wid == 4) wset(W_GO, 2);
    if (LP2) __builtin_amdgcn_s_setprio(LP2);
    const char* p = wsrc4 + 5 * 20480;
    unsigned dst = sm0 + L_RING + RSLOT + (wid & 1) * 10240;
#pragma unroll 1
    for (int st = 5; st < 29; ++st) {
      if (st == 7 + P3REL) { WAITV(0); wset16(wl, st); }
      for (int sp = 0; wget4(W_CONSW) < (st < 7 ? (P3LATE == 3 ? st - 4 : 1) : st - 5) && sp < SPIN_CAP; ++sp) __builtin_amdgcn_s_sleep(1);
      FENCE();
      glds10_sc1(lane * 16, p, dst);
      p = st == 16 ? wsrc5 + 17 * 20480 : p + 20480;
      dst = dst + RSLOT >= sm0 + L_RING + 7 * RSLOT ? dst - 6 * RSLOT : dst + RSLOT;
      WAITV(20);
      if (st >= 7 && (st < 7 + P3REL || st > 8 + P3REL)) wset16(wl, st - 1);
      STAMP(31 + st - 1);
    }
    WAITV(10);
    wset16(wl, 28);
    WAITV(0);
    wset16(wl, 29);
    STAMP(60);
    STAMP_DUMP();
    return;
  }
  if (!isC) {
    if (wid == 6) {
#pragma unroll 1
      for (int k = 0; k < 2; ++k) {
        for (int sp = 0; (k < 1 ? wget4(W_DONE) <= k : wget(W_CNT + 20) < 4) && sp < SPIN_CAP; ++sp) __builtin_amdgcn_s_sleep(2);
        __builtin_amdgcn_s_sleep(PDLY);
        wait_flags(ws + WS_F4 + k * FL + (g * 16 + l15) * 128, epoch);
        wset(W_RDY, k + 3);
        STAMP(22 + k);
      }
    } else {
      for (int sp = 0; wget(W_GO) < 2 && sp < SPIN_CAP; ++sp) __builtin_amdgcn_s_sleep(4);
      for (int sp = 0; wget4(W_CONSW) < TCS && sp < SPIN_CAP; ++sp) __builtin_amdgcn_s_sleep(4);
      FENCE();
      if (lane < 48) {
        f32x4 t = {};
#pragma unroll
        for (int i = 0; i < 27; ++i) t += __builtin_bit_cast(f32x4, LD128(lane * 16, WS_CSUM + (unsigned)(i * 12288 + c * 768)));
#pragma unroll
        for (int e = 0; e < 4; ++e) {
          const unsigned ch = pk2(t[e], t[e]);
          *(LAS u32x4*)(sm + L_WX + lane * 64 + e * 16) = u32x4{ch, pk2(t[e] - bflo(ch), 0.f), 0, 0};
        }
      }
      wset(W_WREADY, 2);
#pragma unroll 1
      for (int k = 0; k < 1; ++k) {
        for (int sp = 0; wget4(W_DONE) <= k && sp < SPIN_CAP; ++sp) __builtin_amdgcn_s_sleep(1);
        FENCE();
        raise(WS_F4 + k * FL + wgid * 128);
        STAMP(22 + k);
      }
      if (b == 0) {
#pragma unroll 1
        for (int i = 0; i < 4; ++i) wait_flags(ws + WS_F2 + (i * 64 + lane) * 128, epoch);
        if (lane == 0) st_flag(ws + WS_EPOCH, epoch);
      }
    }
    STAMP_DUMP();
    return;
  }

  const int wdn = W_DONE + 4 * wid;
  const int wc = W_CONSW + 4 * wid;
  const unsigned lane16 = lane * 16;

  const int r3 = 64 * (c >> 3) + 16 * wid + l15;
  {
    unsigned hpk[10];
    const char* xp = x + (size_t)(128 * g + r3) * 1280 + (c & 7) * 160;
    const u32x4 xr0 = *(const u32x4*)(xp + G * 16), xr1 = *(const u32x4*)(xp + 64 + G * 16);
    const u32x2 xr2 = *(const u32x2*)(xp + 128 + G * 8);
    u32x4 xf[20];
    auto ldo = [&](int s) {
      const unsigned so = WS_O + (unsigned)(g * 80 + 4 * s) * 2048;
      if (s < 16) xf[s] = LD128(G * 2048 + r3 * 16, so);
      else { const u32x2 a = LD64(G * 2048 + r3 * 8, so), d = LD64(G * 2048 + r3 * 8 + 1024, so); xf[s] = u32x4{a[0], a[1], d[0], d[1]}; }
    };
    f32x4 acc[5] = {};
    {
      constexpr int NB = 15;
      bf16x8 wf[NB];
      unsigned so = 0, lv = 0;
      auto rd = [&](int f) {
        if (f == 80) wwait2(W_LANDW, lv, 5);
        if (f % 20 == 0) { so = lane16; OPAQUE(so); so += slot_of(f / 20); OPAQUE(so); }
        wf[f % NB] = ld_b128(sm + so + (f % 20) * 1024);
        if (f == 64) lv = wraw(W_LANDW);
      };
      __builtin_amdgcn_s_sleep(PDLY - 4);
      wwait2(W_LANDW, 0u, 4);
#pragma unroll
      for (int f = 0; f < NB; ++f) rd(f);
      SCHED();
      wait_flags(ws + WS_F2 + (g * 16 + l15) * 128, epoch);
      STAMP(7);
#pragma unroll
      for (int s = 0; s < XW3; ++s) ldo(s);
      SCHED();
#pragma unroll
      for (int f = 0; f < 100; ++f) {
        acc[f % 5] = MFMA16(wf[f % NB], __builtin_bit_cast(bf16x8, xf[f / 5]), acc[f % 5]);
        SCHED();
        if (f + NB < 100) rd(f + NB);
        if (f % X3S == 0 && XW3 + f / X3S < 20) ldo(XW3 + f / X3S);
        if (P3LATE != 1 && f == 3 * (20 - XW3)) wset(wc, P3LATE ? 1 : 1 + P3REL);
        SCHED();
      }
    }
    STAMP(8);
    float s1 = 0.f, s2 = 0.f;
#pragma unroll
    for (int d = 0; d < 10; ++d) {
      const unsigned xv = d < 4 ? xr0[d & 3] : d < 8 ? xr1[d & 3] : xr2[d & 1];
      const unsigned a = pk2(acc[d >> 1][2 * (d & 1)], acc[d >> 1][2 * (d & 1) + 1]);
      const unsigned h = pk2(bflo(a) + bflo(xv), bfhi(a) + bfhi(xv));
      hpk[d] = h;
      s1 += bflo(h) + bfhi(h);
      s2 += bflo(h) * bflo(h) + bfhi(h) * bfhi(h);
    }
    const unsigned hb = WS_H + (unsigned)(g * 80 + 10 * (c & 7)) * 2048;
    ST128((u32x4{hpk[0], hpk[1], hpk[2], hpk[3]}), G * 2048 + r3 * 16, hb);
    ST128((u32x4{hpk[4], hpk[5], hpk[6], hpk[7]}), G * 2048 + r3 * 16, hb + 4 * 2048);
    {
      auto a0 = __builtin_amdgcn_permlane16_swap(hpk[8], hpk[8], false, false);
      auto a1 = __builtin_amdgcn_permlane16_swap(hpk[9], hpk[9], false, false);
      if (!(G & 1)) ST128((u32x4{a0[0], a1[0], a0[1], a1[1]}), (G >> 1) * 2048 + r3 * 16, hb + 8 * 2048);
    }
    s1 = sum32(sum16(s1)); s2 = sum32(sum16(s2));
    if (G == 0) ST64((u32x2{__float_as_uint(s1), __float_as_uint(s2)}), r3 * 8, WS_STAT + (unsigned)(g * 8 + (c & 7)) * 1024);
    if (P3LATE) wset(wc, 1 + P3REL);
    *(LAS u32x4*)(sm + L_OST + wid * 2560 + lane * 16) = u32x4{hpk[0], hpk[1], hpk[2], hpk[3]};
    *(LAS u32x4*)(sm + L_OST + wid * 2560 + 1024 + lane * 16) = u32x4{hpk[4], hpk[5], hpk[6], hpk[7]};
    *(LAS u32x2*)(sm + L_OST + wid * 2560 + 2048 + lane * 8) = u32x2{hpk[8], hpk[9]};
    WAITV(0);
    arrive_w(4, WS_F3);
    STAMP(9);
  }

  {
    constexpr int NB = 6;
    bf16x8 wf[NB];
    unsigned so = 0;
    unsigned lv = 0;
    auto rd = [&](int n) __attribute__((always_inline)) {
      const int part = n / 123, i = n % 123, f = 120 * part + i;
      if (i >= 120) {
        if (n == 120) lds_wait_ge(sm0 + L_W + W_WREADY, 2);
        wf[n % NB] = ld_b128(sm + L_WX + (3 * part + i - 120) * 512 + (lane16 & 511));
      } else {
      if (f % 20 == 0) { wwait2(W_LANDW, lv, 5 + f / 20 + 1); so = lane16; OPAQUE(so); so += slot_of(5 + f / 20); OPAQUE(so); }
      wf[n % NB] = ld_b128(sm + so + (f % 20) * 1024);
      if (f % 20 == 12) lv = wraw(W_LANDW);
      if (f % 20 == 19 && f / 20 < 12 - P4H) wset(wc, 5 + f / 20 + 2);
      }
    };
    __builtin_amdgcn_s_sleep(PDLY - 2);
#pragma unroll
    for (int n = 0; n < NB; ++n) rd(n);
    SCHED();
    wait_flags(ws + WS_F3 + (g * 16 + l15) * 128, epoch);
    STAMP(10);
    const int r4 = 32 * wid + l31;
    const unsigned vx = hi * 2048 + r4 * 16;
    const unsigned hb = WS_H + (unsigned)(g * 80) * 2048;
    u32x4 xf[40];
    u32x2 stp[4];
    constexpr int XW = XW4;
#pragma unroll
    for (int i = 0; i < XW; ++i) xf[i] = LD128(vx, hb + i * 4096);
    SCHED();
    float r2 = 0.f;
    unsigned xe0 = 0, xe1 = 0;
    static_for<0, 2>([&](auto pc) __attribute__((always_inline)) {
      constexpr int part = pc.value;
      f32x16 acc[3] = {};
      static_for<0, 123>([&](auto ic) __attribute__((always_inline)) {
        constexpr int i = ic.value;
        const int n = 123 * part + i;
        if (i % 20 == 0 && n > 0 && i < 120) { STAMP2(42 + (120 * part + i) / 20); SCHED(); }
        acc[i % 3] = MFMA32(wf[n % NB], __builtin_bit_cast(bf16x8, i < 120 ? xf[i < 120 ? i / 3 : 0] : u32x4{xe0, xe1, 0, 0}), acc[i % 3]);
        SCHED();
        if (n + NB < 246) rd(n + NB);
        if (part == 0 && (i & 1) && (i >> 1) + XW < 40) xf[(i >> 1) + XW] = LD128(vx, hb + ((i >> 1) + XW) * 4096);
        if (n >= 4 && n < 12 && !(n & 1)) stp[(n - 4) >> 1] = LD64(hi * 4096 + r4 * 8, WS_STAT + (unsigned)(g * 8 + ((n - 4) >> 1)) * 1024);
        if (n == 44) {
          float s1 = 0.f, s2 = 0.f;
#pragma unroll
          for (int j = 0; j < 4; ++j) { s1 += __uint_as_float(stp[j][0]); s2 += __uint_as_float(stp[j][1]); }
          s1 = sum32(s1) * (1.f / 640.f); s2 = sum32(s2) * (1.f / 640.f);
          r2 = __builtin_amdgcn_rcpf(fmaxf(s2 - s1 * s1, 0.f) + 1e-5f);
          const unsigned mh = pk2(-s1, 0.f);
          xe0 = hi ? 0u : pk2(-s1, -s1 - bflo(mh));
          xe1 = hi ? 0u : mh;
          OPAQUE(r2); OPAQUE(xe0); OPAQUE(xe1);
        }
        SCHED();
        if (part == 1 && i == 36) { WAITV(0); wset(wdn, 1); SCHED(); }
      });
      STAMP2(54 + part);
      if (part == 1) STAMP(11);
#pragma unroll
      for (int t = 0; t < 3; ++t) {
        unsigned o[4];
#pragma unroll
        for (int d = 0; d < 4; ++d) {
          const f32x2 hv = (f32x2{acc[t][2 * d], acc[t][2 * d + 1]} * r2) * f32x2{acc[t][8 + 2 * d], acc[t][8 + 2 * d + 1]};
          o[d] = pk2(hv[0], hv[1]);
        }
        ST128((u32x4{o[0], o[1], o[2], o[3]}), vx, WS_HH + (unsigned)(g * 192 + 96 * part + 6 * c + 2 * t) * 2048);
      }
      SCHED();
    });
    if (P4H) wset(wc, 18);
    STAMP(12);
  }

  {
    unsigned ln5 = lane16;
    OPAQUE(ln5);
    const int wk = wid & 1;
    const unsigned hb5 = WS_HH + (unsigned)(g * 192) * 2048 + wk * 16384;
    const unsigned v50 = (ln5 >> 8) * 2048 + (64 * (c >> 3) + 16 * wid) * 16 + (ln5 & 255), v51 = v50 ^ 256;
    f32x4 acc[5] = {}, acp[5] = {};
    constexpr int NB = 8;
    bf16x8 wf[NB];
    u32x4 x5[24];
    unsigned so = 0;
    unsigned lv = 0;
    auto rd = [&](int e) {
      if (e % 10 == 0) { wwait2(W_LANDW, lv, 17 + e / 10 + 1); so = ln5 + wk * 10240; OPAQUE(so); so += slot_of(17 + e / 10); OPAQUE(so); }
      wf[e % NB] = ld_b128(sm + so + (e % 10) * 1024);
      if (e % 10 == 3) lv = wraw(W_LANDW);
      if (e % 10 == 9) wset(wc, 17 + e / 10 + 2);
    };
    auto ldx = [&](int part, int j) {
      const int kk = 12 * part + (j >> 1);
      x5[j] = LD128((j & 1) ? v51 : v50, hb5 + (4 * (kk >> 1) + (kk & 1)) * 8192);
    };
    lds_wait_ge(sm0 + L_W + W_RDY, 3);
    STAMP(40);
#pragma unroll
    for (int j = 0; j < XW5I; ++j) ldx(0, j);
#pragma unroll
    for (int e = 0; e < NB; ++e) rd(e);
    SCHED();
    WAITV(XW5I);
    arrive_w(5, WS_F4 + FL);
    STAMP(13);
#pragma unroll
    for (int j = XW5I; j < XW5; ++j) ldx(0, j);
    SCHED();
#pragma unroll
    for (int part = 0; part < 2; ++part) {
#pragma unroll
      for (int i = 0; i < 60; ++i) {
        const int e = 60 * part + i;
        acc[i % 5] = MFMA16(wf[e % NB], __builtin_bit_cast(bf16x8, x5[2 * (i / 5)]), acc[i % 5]);
        SCHED();
        acp[i % 5] = MFMA16(wf[e % NB], __builtin_bit_cast(bf16x8, x5[2 * (i / 5) + 1]), acp[i % 5]);
        SCHED();
        if (e + NB < 120) rd(e + NB);
        if ((part ? XW5B : XW5) + i < 24) ldx(part, (part ? XW5B : XW5) + i);
        SCHED();
      }
      if (part == 0) {
        lds_wait_ge(sm0 + L_W + W_RDY, 4);
        STAMP(41);
#pragma unroll
        for (int j = 0; j < XW5B; ++j) ldx(1, j);
        SCHED();
      }
    }
    for (int sp = 0; wget4(W_CONSW) < 24 && sp < SPIN_CAP; ++sp) {}
    FENCE();
#pragma unroll
    for (int tt = 0; tt < 5; ++tt) *(LAS f32x4*)(sm + slot_of(22) + (wid ^ 1) * 5120 + tt * 1024 + ln5) = acp[tt];
    wset(wdn, 9);
    lds_wait_ge(sm0 + L_W + W_DONE + 4 * (wid ^ 1), 9);
#pragma unroll
    for (int tt = 0; tt < 5; ++tt) acc[tt] += *(LAS f32x4*)(sm + slot_of(22) + wid * 5120 + tt * 1024 + ln5);
    STAMP(14);
    unsigned opk[10];
    const u32x4 h0 = *(LAS u32x4*)(sm + L_OST + wid * 2560 + ln5), h1 = *(LAS u32x4*)(sm + L_OST + wid * 2560 + 1024 + ln5);
    const u32x2 h2 = *(LAS u32x2*)(sm + L_OST + wid * 2560 + 2048 + (ln5 >> 1));
#pragma unroll
    for (int d = 0; d < 10; ++d) {
      const unsigned hv = d < 4 ? h0[d & 3] : d < 8 ? h1[d & 3] : h2[d & 1];
      const unsigned a = pk2(acc[d >> 1][2 * (d & 1)], acc[d >> 1][2 * (d & 1) + 1]);
      opk[d] = pk2(bflo(a) + bflo(hv), bfhi(a) + bfhi(hv));
    }
    const unsigned G5 = ln5 >> 8;
    char* op = out + (size_t)(128 * g + 64 * (c >> 3) + 16 * wid) * 1280 + (c & 7) * 160 + (ln5 & 255) * 80;
    *(u32x4*)(op + G5 * 16) = u32x4{opk[0], opk[1], opk[2], opk[3]};
    *(u32x4*)(op + 64 + G5 * 16) = u32x4{opk[4], opk[5], opk[6], opk[7]};
    *(u32x2*)(op + 128 + G5 * 8) = u32x2{opk[8], opk[9]};
    if (OWB) asm volatile("s_waitcnt vmcnt(0)\n\tbuffer_wbl2 sc1" ::: "memory");
    STAMP(15);
    STAMP_DUMP();
  }
}

extern "C" void kernel_launch(void* const* d_in, const int* in_sizes, int n_in, void* d_out, int out_size, void* d_ws, size_t ws_size,
                              hipStream_t stream) {
  fused_block<<<256, 512, 0, stream>>>((const char*)d_in[0], (const char*)d_in[1], (const char*)d_in[2], (const char*)d_in[4],
                                       (const char*)d_in[5], (const char*)d_in[6], (char*)d_out, (char*)d_ws);
}
```

```cpp
#include <hip/hip_runtime.h>
#include <stdint.h>

typedef unsigned long long u64;
typedef __attribute__((ext_vector_type(8))) short bf16x8;
typedef __attribute__((ext_vector_type(4))) short s16x4;
typedef __attribute__((ext_vector_type(2))) __bf16 bf16x2;
typedef __attribute__((ext_vector_type(16))) float f32x16;
typedef __attribute__((ext_vector_type(4))) float f32x4;
typedef __attribute__((ext_vector_type(2))) float f32x2;
typedef __attribute__((ext_vector_type(4))) unsigned u32x4;
typedef __attribute__((ext_vector_type(2))) unsigned u32x2;
#define LAS __attribute__((address_space(3)))
#define DI __device__ __forceinline__

constexpr unsigned WS_EPOCH = 0, WS_HINT = 1024;
constexpr unsigned FL = 256 * 128;
constexpr unsigned WS_F0 = 4096, WS_F1 = WS_F0 + FL, WS_F2 = WS_F1 + FL, WS_F3 = WS_F2 + FL, WS_F4 = WS_F3 + FL;
constexpr unsigned WS_F1P = WS_F4 + 2 * FL;
constexpr unsigned WS_CSUM = WS_F1P + 2048;
constexpr unsigned WS_ST1 = WS_CSUM + 27 * 12288;
constexpr unsigned WS_CS1 = WS_ST1 + 2048 * 16;
constexpr unsigned WS_STAT = WS_CS1 + 16 * 16 * 512;
constexpr unsigned WS_QKV = 1u << 20;
constexpr unsigned WS_O = WS_QKV + 16 * 240 * 2048;
constexpr unsigned WS_H = WS_O + 16 * 80 * 2048;
constexpr unsigned WS_HH = WS_H + 16 * 80 * 2048;
constexpr unsigned WS_W3 = WS_HH + 16 * 192 * 2048;
constexpr unsigned WS_W4 = WS_W3 + 8 * 100 * 1024;
constexpr unsigned WS_W5 = WS_W4 + 16 * 240 * 1024;
constexpr unsigned WS_END = WS_W5 + 8 * 240 * 1024;
constexpr unsigned WS_DIAG = 32u << 20;
static_assert(WS_STAT + 16 * 8 * 1024 <= WS_QKV && WS_END <= WS_DIAG, "workspace map");

constexpr unsigned L_STAT = 0, L_CS = 1024;
constexpr unsigned L_W = 1536;
constexpr unsigned L_ONES = 1792;
constexpr unsigned L_KT = 2304;
constexpr unsigned L_RP = 2560;
constexpr unsigned L_OST = 2560;
constexpr unsigned L_JUNK = 31488;
constexpr unsigned L_HSTG = 81920;
constexpr unsigned L_WX = 12800;
constexpr int CODE_LINES = 54;
constexpr unsigned L_P1 = 32768;
constexpr unsigned L_K = 12800, L_V = L_K + 5 * 6144, VSTR = 6208;
constexpr unsigned L_RING = 20480, RSLOT = 20480;
constexpr unsigned slot_of(int i) { return L_RING + ((i + 3) % 7) * RSLOT; }
static_assert(L_V + 5 * VSTR <= L_RING + 3 * RSLOT, "LDS map");
enum { W_LAND1 = 0, W_LANDW = 8, W_CONS1 = 16, W_CONSW = 32, W_GO = 48, W_S1DONE = 52, W_STATS = 56, W_WREADY = 60, W_RDY = 64, W_DONE = 80, W_CNT = 96, W_INIT = 128, W_EPO = 176, W_LL = 184 };

constexpr int PDLY = 24, GDLY = 16, LDLY = 16;
constexpr int XW3 = 5, XW4 = 9, XW5 = 6;
constexpr int P3REL = 3;
constexpr float QSCALE = 0.22360679774997896f * 1.4426950408889634f;

#define MFMA32(a, b, c) __builtin_amdgcn_mfma_f32_32x32x16_bf16((a), (b), (c), 0, 0, 0)
#define MFMA16(a, b, c) __builtin_amdgcn_mfma_f32_16x16x32_bf16((a), (b), (c), 0, 0, 0)
#define BAR() asm volatile("s_waitcnt lgkmcnt(0)\n\ts_barrier" ::: "memory")
#define SCHED() __builtin_amdgcn_sched_barrier(0)
#define OPAQUE(v) asm volatile("" : "+v"(v))
#define SGB(m, n) __builtin_amdgcn_sched_group_barrier((m), (n), 0)
#define WAITV(n) asm volatile("s_waitcnt vmcnt(%0)" ::"i"(n) : "memory")

#define STAMP(i) do {} while (0)
#define STAMP2(i) do {} while (0)
#define STAMP_DUMP() do {} while (0)

DI void glds(unsigned voff, const char* sbase, unsigned ldst) {
  unsigned sv;
  asm volatile("s_mov_b32 %0, m0\n\ts_mov_b32 m0, %3\n\ts_nop 2\n\tglobal_load_lds_dwordx4 %1, %2 nt\n\ts_mov_b32 m0, %0"
               : "=&s"(sv) : "v"(voff), "s"(sbase), "s"(ldst) : "memory");
}
DI void glds5_sc1(unsigned voff, const char* sbase, unsigned ldst) {
  unsigned sv;
  asm volatile("s_mov_b32 %0, m0\n\ts_mov_b32 m0, %3\n\ts_nop 2\n\t"
               "global_load_lds_dwordx4 %1, %2 offset:-2048 sc1\n\tglobal_load_lds_dwordx4 %1, %2 offset:-1024 sc1\n\t"
               "global_load_lds_dwordx4 %1, %2 sc1\n\tglobal_load_lds_dwordx4 %1, %2 offset:1024 sc1\n\t"
               "global_load_lds_dwordx4 %1, %2 offset:2048 sc1\n\ts_mov_b32 m0, %0"
               : "=&s"(sv) : "v"(voff), "s"(sbase + 2048), "s"(ldst + 2048) : "memory");
}
DI void glds5(unsigned voff, const char* sbase, unsigned ldst) {
  unsigned sv;
  asm volatile("s_mov_b32 %0, m0\n\ts_mov_b32 m0, %3\n\ts_nop 2\n\t"
               "global_load_lds_dwordx4 %1, %2 offset:-2048 nt\n\tglobal_load_lds_dwordx4 %1, %2 offset:-1024 nt\n\t"
               "global_load_lds_dwordx4 %1, %2 nt\n\tglobal_load_lds_dwordx4 %1, %2 offset:1024 nt\n\t"
               "global_load_lds_dwordx4 %1, %2 offset:2048 nt\n\ts_mov_b32 m0, %0"
               : "=&s"(sv) : "v"(voff), "s"(sbase + 2048), "s"(ldst + 2048) : "memory");
}
DI void glds8(const unsigned (&v)[8], const char* sbase, unsigned ldst) {
  unsigned sv;
  asm volatile("s_mov_b32 %0, m0\n\ts_mov_b32 m0, %10\n\ts_nop 2\n\t"
               "global_load_lds_dwordx4 %1, %9 offset:-4096\n\tglobal_load_lds_dwordx4 %2, %9 offset:-3072\n\t"
               "global_load_lds_dwordx4 %3, %9 offset:-2048\n\tglobal_load_lds_dwordx4 %4, %9 offset:-1024\n\t"
               "global_load_lds_dwordx4 %5, %9\n\tglobal_load_lds_dwordx4 %6, %9 offset:1024\n\t"
               "global_load_lds_dwordx4 %7, %9 offset:2048\n\tglobal_load_lds_dwordx4 %8, %9 offset:3072\n\ts_mov_b32 m0, %0"
               : "=&s"(sv) : "v"(v[0]), "v"(v[1]), "v"(v[2]), "v"(v[3]), "v"(v[4]), "v"(v[5]), "v"(v[6]), "v"(v[7]),
                 "s"(sbase), "s"(ldst + 4096) : "memory");
}
DI void glds10_sc1(unsigned voff, const char* sbase, unsigned ldst) {
  unsigned sv;
  asm volatile("s_mov_b32 %0, m0\n\ts_mov_b32 m0, %4\n\ts_nop 2\n\t"
               "global_load_lds_dwordx4 %1, %3 offset:-4096 sc1\n\tglobal_load_lds_dwordx4 %1, %3 offset:-3072 sc1\n\t"
               "global_load_lds_dwordx4 %1, %3 offset:-2048 sc1\n\tglobal_load_lds_dwordx4 %1, %3 offset:-1024 sc1\n\t"
               "global_load_lds_dwordx4 %1, %3 sc1\n\tglobal_load_lds_dwordx4 %1, %3 offset:1024 sc1\n\t"
               "global_load_lds_dwordx4 %1, %3 offset:2048 sc1\n\tglobal_load_lds_dwordx4 %1, %3 offset:3072 sc1\n\t"
               "s_add_u32 m0, m0, 0x1000\n\ts_nop 0\n\t"
               "global_load_lds_dwordx4 %2, %3 sc1\n\tglobal_load_lds_dwordx4 %2, %3 offset:1024 sc1\n\ts_mov_b32 m0, %0"
               : "=&s"(sv) : "v"(voff), "v"(voff + 4096), "s"(sbase + 4096), "s"(ldst + 4096) : "memory", "scc");
}
DI void glds2_sc1(unsigned voff, const char* sbase, unsigned ldst) {
  unsigned sv;
  asm volatile("s_mov_b32 %0, m0\n\ts_mov_b32 m0, %3\n\ts_nop 2\n\t"
               "global_load_lds_dwordx4 %1, %2 sc1\n\tglobal_load_lds_dwordx4 %1, %2 offset:1024 sc1\n\ts_mov_b32 m0, %0"
               : "=&s"(sv) : "v"(voff), "s"(sbase), "s"(ldst) : "memory");
}
#define ST128(v, voff, soff) __builtin_amdgcn_raw_buffer_store_b128((v), rs, (unsigned)(voff) + (unsigned)(soff), 0, 16)
#define ST64(v, voff, soff) __builtin_amdgcn_raw_buffer_store_b64((v), rs, (unsigned)(voff) + (unsigned)(soff), 0, 16)
#define LD128(voff, soff) __builtin_amdgcn_raw_buffer_load_b128(rs, (unsigned)(voff), (unsigned)(soff), 16)
#define LD64(voff, soff) __builtin_amdgcn_raw_buffer_load_b64(rs, (unsigned)(voff), (unsigned)(soff), 16)
DI bf16x8 ld_b128(LAS char* p) { return *(LAS bf16x8*)p; }
DI s16x4 ld_tr(LAS char* p) { return __builtin_amdgcn_ds_read_tr16_b64_v4i16((LAS s16x4*)p); }
DI bf16x8 ld_tr2(LAS char* p0, LAS char* p1) {
  s16x4 a = ld_tr(p0), b = ld_tr(p1);
  return __builtin_shufflevector(a, b, 0, 1, 2, 3, 4, 5, 6, 7);
}
DI unsigned pk2(float lo, float hi) {
  f32x2 v = {lo, hi};
  return __builtin_bit_cast(unsigned, __builtin_convertvector(v, bf16x2));
}
DI float bflo(unsigned w) { return __uint_as_float(w << 16); }
DI float bfhi(unsigned w) { return __uint_as_float(w & 0xffff0000u); }
DI float sum32(float v) {
  auto r = __builtin_amdgcn_permlane32_swap(__float_as_uint(v), __float_as_uint(v), false, false);
  return __uint_as_float(r[0]) + __uint_as_float(r[1]);
}
DI float sum16(float v) {
  auto r = __builtin_amdgcn_permlane16_swap(__float_as_uint(v), __float_as_uint(v), false, false);
  return __uint_as_float(r[0]) + __uint_as_float(r[1]);
}
DI float sum8(float v) {
  v += __uint_as_float(__builtin_amdgcn_update_dpp(0, __float_as_uint(v), 0xB1, 0xf, 0xf, true));
  v += __uint_as_float(__builtin_amdgcn_update_dpp(0, __float_as_uint(v), 0x4E, 0xf, 0xf, true));
  v += __uint_as_float(__builtin_amdgcn_update_dpp(0, __float_as_uint(v), 0x141, 0xf, 0xf, true));
  return v;
}
DI float max32(float v) {
  auto r = __builtin_amdgcn_permlane32_swap(__float_as_uint(v), __float_as_uint(v), false, false);
  return fmaxf(__uint_as_float(r[0]), __uint_as_float(r[1]));
}
DI u64 mix64(u64 z) {
  z += 0x9E3779B97F4A7C15ull; z = (z ^ (z >> 30)) * 0xBF58476D1CE4E5B9ull; z = (z ^ (z >> 27)) * 0x94D049BB133111EBull;
  return z ^ (z >> 31);
}
DI u64 ld_flag(const char* p) { return __hip_atomic_load((const u64*)p, __ATOMIC_RELAXED, __HIP_MEMORY_SCOPE_AGENT); }
DI void st_flag(char* p, u64 v) { __hip_atomic_store((u64*)p, v, __ATOMIC_RELAXED, __HIP_MEMORY_SCOPE_AGENT); }
DI void wait_flags_from(const char* p, u64 v, u64 epoch) {
  for (unsigned spins = 0; spins < (1u << 18); ++spins) {
    if (__all(v == epoch)) break;
    __builtin_amdgcn_s_sleep(1);
    v = ld_flag(p);
  }
  asm volatile("" ::: "memory");
}
DI void wait_flags(const char* p, u64 epoch) { wait_flags_from(p, ld_flag(p), epoch); }
DI f32x16 mfma32_c(bf16x8 a, bf16x8 b, const f32x16& c) {
  f32x16 d;
  asm volatile("s_nop 1\n\tv_mfma_f32_32x32x16_bf16 %0, %1, %2, %3" : "=&v"(d) : "v"(a), "v"(b), "v"(c));
  return d;
}

DI float dot2(unsigned a, unsigned b, float c) {
  return __builtin_amdgcn_fdot2_f32_bf16(__builtin_bit_cast(bf16x2, a), __builtin_bit_cast(bf16x2, b), c, false);
}
DI int rfl(int v) { return __builtin_amdgcn_readfirstlane(v); }
DI int mini(int a, int b) { return a < b ? a : b; }
typedef __attribute__((ext_vector_type(4))) int i32x4;
template <int I> struct IC { static constexpr int value = I; };
template <int I, int N, class F> DI void static_for(F&& f) { if constexpr (I < N) { f(IC<I>{}); static_for<I + 1, N>(f); } }
#define FENCE() asm volatile("" ::: "memory")
constexpr int SPIN_CAP = 1 << 22;
DI void lds_wait_eq(unsigned v, unsigned addr, unsigned want) {
  unsigned s0, cnt;
  asm volatile("s_mov_b32 %2, 0x400000\n"
               "1:\n\tv_readfirstlane_b32 %1, %0\n\ts_cmp_eq_u32 %1, %4\n\ts_cbranch_scc1 2f\n\t"
               "ds_read_b32 %0, %3\n\ts_sub_u32 %2, %2, 1\n\ts_waitcnt lgkmcnt(0)\n\ts_cbranch_scc0 1b\n"
               "2:"
               : "+v"(v), "=&s"(s0), "=&s"(cnt) : "v"(addr), "s"(want) : "memory", "scc");
}
DI void lds_wait_ge(unsigned addr, unsigned need) {
  unsigned v, s0, cnt;
  asm volatile("s_mov_b32 %2, 0x400000\n"
               "1:\n\tds_read_b32 %0, %3\n\ts_waitcnt lgkmcnt(0)\n\tv_readfirstlane_b32 %1, %0\n\ts_cmp_ge_u32 %1, %4\n\ts_cbranch_scc1 2f\n\t"
               "s_sub_u32 %2, %2, 1\n\ts_cbranch_scc0 1b\n"
               "2:"
               : "=&v"(v), "=&s"(s0), "=&s"(cnt) : "v"(addr), "s"(need) : "memory", "scc");
}
DI void lds_wait_halves(unsigned v, unsigned addr, unsigned need) {
  unsigned s0, s1, cnt;
  asm volatile("s_mov_b32 %3, 0x400000\n"
               "1:\n\tv_readfirstlane_b32 %1, %0\n\ts_lshr_b32 %2, %1, 16\n\ts_and_b32 %1, %1, 0xffff\n\ts_min_u32 %1, %1, %2\n\t"
               "s_cmp_ge_u32 %1, %5\n\ts_cbranch_scc1 2f\n\t"
               "ds_read_b32 %0, %4\n\ts_sub_u32 %3, %3, 1\n\ts_waitcnt lgkmcnt(0)\n\ts_cbranch_scc0 1b\n"
               "2:"
               : "+v"(v), "=&s"(s0), "=&s"(s1), "=&s"(cnt) : "v"(addr), "s"(need) : "memory", "scc");
}

__global__ __launch_bounds__(512, 2) void fused_block(const char* __restrict__ x, const char* __restrict__ wqkv, const char* __restrict__ wo,
                                                      const char* __restrict__ wg, const char* __restrict__ wu, const char* __restrict__ wd,
                                                      char* __restrict__ out, char* ws) {
  __shared__ __attribute__((aligned(16))) char smem_[163840];
  LAS char* sm = (LAS char*)smem_;
  const unsigned sm0 = (unsigned)(uintptr_t)sm;
  const int tid = threadIdx.x, lane = tid & 63;
  const int wid = __builtin_amdgcn_readfirstlane(tid >> 6);
  STAMP(0);
  const int b = blockIdx.x, g = 4 * ((b & 7) >> 1) + (b >> 6), c = 8 * (b & 1) + ((b >> 3) & 7);
  const int wgid = g * 16 + c;
  const int g2 = b >> 4, c2 = 2 * (b & 7) + ((b >> 3) & 1);
  const int c1 = ((g & 1) && (c & 7) == 7) ? c ^ 8 : c;
  const bool lx = !(wid & 1);
  unsigned vo[8];
  {
    const unsigned rsh = lx ? 3 : 4, rstr = lx ? 1280 : 3840, pstr = (lx ? 8 * 1280 : 4 * 3840) - 1024;
    const unsigned cm = lx ? 7 : 15, csh = lx ? 0 : 2, chx = lx ? 64 : 0;
    const unsigned ch0 = (lane & cm) ^ ((lane >> 4) << csh);
    const unsigned vb = (lane >> rsh) * rstr + 4096;
#pragma unroll
    for (int i = 0; i < 8; ++i) vo[i] = vb + i * pstr + ((ch0 << 4) ^ ((i & 1) ? chx : 0));
  }
  const char* const rsb = (lx ? x : wqkv) + (lx ? g * (128 * 1280) : c1 * 256);
  const unsigned hstep = lx ? 64 * 1280 : 32 * 3840, sstep = lx ? 128 : 64 * 3840, rdst = sm0 + L_P1 + (wid & 1) * 16384;
  if (c1 < 15 && wid < 4) {
#pragma unroll
    for (int s = 0; s < 2; ++s) glds8(vo, rsb + s * sstep + (wid >> 1) * hstep, rdst + s * 32768 + (wid >> 1) * 8192);
  }
  SCHED();
  const auto rs = __builtin_amdgcn_make_buffer_rsrc(ws, 0, WS_END, 0x00020000);
  const bool isC = wid < 4, isL = (wid >> 1) == 2;
  const int l31 = lane & 31, hi = lane >> 5, G = lane >> 4, q4 = (lane & 15) >> 2, p4 = lane & 3, l15 = lane & 15;
  STAMP(6);
  u64 eraw = 0, epoch = 0;
  auto load_epoch = [&]() { unsigned z = 0; OPAQUE(z); eraw = ld_flag(ws + WS_EPOCH + z); };
  auto get_epoch = [&]() {
    const u64 e = mix64(eraw);
    const unsigned e0 = __builtin_amdgcn_readfirstlane((unsigned)e), e1 = __builtin_amdgcn_readfirstlane((unsigned)(e >> 32));
    epoch = ((u64)e1 << 32) | e0;
  };
  auto wset = [&](int w, int v) { FENCE(); *(volatile LAS int*)(sm + L_W + w) = v; FENCE(); };
  auto wget = [&](int w) { return rfl(*(volatile LAS int*)(sm + L_W + w)); };
  auto wget4 = [&](int w) { const i32x4 v = *(volatile LAS i32x4*)(sm + L_W + w); return rfl(mini(mini(v[0], v[1]), mini(v[2], v[3]))); };
  auto wwait = [&](int w, int need) { for (int sp = 0; wget(w) < need && sp < SPIN_CAP; ++sp) __builtin_amdgcn_s_sleep(1); FENCE(); };
  auto wraw = [&](int w) { return *(volatile LAS unsigned*)(sm + L_W + w); };
  auto wset16 = [&](int w, int v) { FENCE(); *(volatile LAS short*)(sm + L_W + w) = (short)v; FENCE(); };
  auto wwait2 = [&](int w, unsigned first, int need) { lds_wait_halves(first, sm0 + L_W + w, need); };
  auto count = [&](int k) {
    unsigned old = 0;
    if (lane == 0) old = __hip_atomic_fetch_add((LAS unsigned*)(sm + L_W + W_CNT + 4 * k), 1u, __ATOMIC_RELAXED, __HIP_MEMORY_SCOPE_WORKGROUP);
    return rfl(old);
  };
  auto raise = [&](unsigned off) { if (lane < 16) st_flag(ws + off + lane * 8, epoch); };
  auto arrive_n = [&](int k, unsigned flag_base, int n) {
    if (count(k) == n - 1) {
      raise(flag_base + (g * 16 + c1) * 128);
      if (k == 1 && c1 == 14) raise(WS_HINT + g * 128);
    }
  };
  if (wid == 7) {
    u64 pc;
    asm volatile("s_getpc_b64 %0" : "=s"(pc));
    unsigned sv;
    const int ln = lane < CODE_LINES ? lane : 0;
    asm volatile("s_mov_b32 %0, m0\n\ts_mov_b32 m0, %3\n\ts_nop 2\n\tglobal_load_lds_dword %1, %2\n\ts_mov_b32 m0, %0"
                 : "=&s"(sv) : "v"((((b >> 3) & 7) * CODE_LINES + ln) * 128), "s"(pc), "s"(sm0 + L_JUNK) : "memory");
  }
  if (wid == 6) *(LAS unsigned*)(sm + L_W + 4 * lane) = 0;
  if (wid == 7) *(LAS u32x2*)(sm + L_ONES + 8 * lane) = u32x2{0x3f803f80u, 0x3f803f80u};
  if (wid == 6 && lane < 12) *(LAS u32x4*)(sm + L_KT + 16 * lane) = u32x4{lane < 8 - 4 * g2 ? 0x3f803f80u : 0x00003f80u, 0, 0, 0};

  auto arrive_w = [&](int k, unsigned flag_base) {
    if (count(k) == 3) raise(flag_base + wgid * 128);
  };
  bf16x8 rf[10];
  unsigned rvo = 0, rvo1 = 0, rstep5 = 0;
  int rn = 0, roct = 0;
  auto wide_read = [&](unsigned st, unsigned dst, unsigned nk5) {
    LAS char* p = sm + st + q4 * 1280 + G * 320;
#pragma unroll
    for (int j = 0; j < 10; ++j) {
      const int tt = j % 5;
      LAS char* pj = p + (j / 5) * 160 + (tt < 4 ? 64 * (tt >> 1) + 16 * p4 + 8 * (tt & 1) : 128 + 8 * p4);
      rf[j] = ld_tr2(pj, pj + 4 * 1280);
    }
    rvo = rvo1 = dst + G * 2 * nk5 * 1024 + l15 * 16; rstep5 = nk5 * 1024; rn = 10;
  };
  auto retile_store = [&](int j) {
    const int o = roct + (j >> 1);
    ST128(__builtin_bit_cast(u32x4, rf[j]), (j & 1) ? rvo1 : rvo,
          rn == 10 ? (j % 5) * 1024 + (j / 5) * rstep5 : (o >> 1) * 3072 + (o & 1) * 512);
  };
  const int su = wgid * 3 + wid - 5;
  const unsigned rst = L_RP + (wid == 4 ? 0 : 10240 + (wid - 5) * 6144);
  auto late_load = [&](int w) {
    const unsigned rsw = sm0 + L_RP + (w == 4 ? 0 : 10240 + (w - 5) * 6144);
    const int suw = wgid * 3 + w - 5;
    if (w == 4) {
      if (wgid < 192) {
        glds5(lane * 16, wd + (size_t)wgid * 10240, rsw);
        glds5(lane * 16, wd + (size_t)wgid * 10240 + 5120, rsw + 5120);
      }
    } else if (suw < 648) {
      const int cl = suw % 24, rgp = suw / 24;
      const unsigned vr = (lane >> 3) * 3072 + (lane & 7) * 16;
#pragma unroll
      for (int i = 0; i < 3; ++i) {
        const size_t so = (size_t)mini(3 * rgp + i, 79) * 8 * 3072 + cl * 128;
        glds(vr, wg + so, rsw + i * 1024);
        glds(vr, wu + so, rsw + 3072 + i * 1024);
      }
    }
  };
  auto late_read = [&]() {
    WAITV(0);
    if (wid == 4) {
      const int pos = 96 * ((wgid % 12) / 6) + 6 * (wgid / 12) + wgid % 6;
      if (wgid < 192) wide_read(rst, WS_W5 + (pos >> 2) * 5120 + (pos & 3) * 256, 240);
    } else if (su < 648) {
      const int cl = su % 24, rgp = su / 24;
      float cs[2] = {0.f, 0.f};
      rn = rgp == 26 ? 4 : 6;
#pragma unroll
      for (int j = 0; j < 6; ++j) {
        LAS char* p = sm + rst + (G & 1) * 3072 + ((j >> 1) * 8 + q4) * 128 + (2 * (j & 1) + hi) * 32 + 16 * (p4 & 1) + 8 * (p4 >> 1);
        rf[j] = ld_tr2(p, p + 4 * 128);
        const u32x4 w = __builtin_bit_cast(u32x4, rf[j]);
        const unsigned one = j < rn ? 0x3f803f80u : 0u;
#pragma unroll
        for (int d = 0; d < 4; ++d) cs[j & 1] = dot2(w[d], one, cs[j & 1]);
      }
      {
        const unsigned t0 = 4 * cl + hi, t1 = t0 + 2;
        rvo = WS_W4 + ((t0 / 3) * 120 + t0 % 3) * 1024 + l31 * 16;
        rvo1 = WS_W4 + ((t1 / 3) * 120 + t1 % 3) * 1024 + l31 * 16;
      }
      roct = 3 * rgp;
      const unsigned cvo = lane * 4;
#pragma unroll
      for (int jp = 0; jp < 2; ++jp)
        __builtin_amdgcn_raw_buffer_store_b32(__float_as_uint(cs[jp]), rs, cvo + WS_CSUM + (unsigned)(rgp * 12288 + cl * 512 + jp * 256), 0, 16);
    }
    FENCE();
    if (lane == 0) __hip_atomic_fetch_add((LAS unsigned*)(sm + L_W + W_S1DONE), 1u, __ATOMIC_RELAXED, __HIP_MEMORY_SCOPE_WORKGROUP);
    FENCE();
  };
  if (isL && c1 < 15) {
    unsigned vx[8], vw[8];
#pragma unroll
    for (int i = 0; i < 8; ++i) {
      vx[i] = (lane >> 3) * 1280 + 4096 + i * (8 * 1280 - 1024) + ((((lane & 7) ^ (lane >> 4)) << 4) ^ ((i & 1) * 64));
      vw[i] = (lane >> 4) * 3840 + 4096 + i * (4 * 3840 - 1024) + (((lane & 15) ^ ((lane >> 4) << 2)) << 4);
    }
    int s = 2 + (wid & 1);
    BAR();
#pragma unroll 1
    for (; s < 10; s += 2) {
      const char* px = x + g * (128 * 1280) + s * 128;
      const char* pw = wqkv + c1 * 256 + s * (64 * 3840);
      for (int sp = 0; wget4(W_CONS1) < s - 3 && sp < SPIN_CAP; ++sp) {}
      STAMP2(7 + s / 2);
      FENCE();
      const unsigned d = sm0 + L_P1 + (s & 3) * 32768;
      glds8(vx, px, d);
      glds8(vw, pw, d + 16384);
      WAITV(16);
      if (s >= 4) wset(W_INIT + 4 * (s - 2), 0x01010101);
      STAMP(16 + s - 2);
      glds8(vx, px + 64 * 1280, d + 8192);
      glds8(vw, pw + 32 * 3840, d + 24576);
    }
    WAITV(0);
    wset(W_INIT + 4 * (s - 2), 0x01010101);
    STAMP(24);
    load_epoch();
    get_epoch();
    wwait(wid == 4 ? W_STATS : W_LL, 1);
    late_read();
  } else if (isC && c1 < 15) {
    const int wn = wid & 1, wt = wid >> 1;
    f32x16 acc[2][2] = {};
    const unsigned ax = L_P1 + (64 * wt + l31) * 128 + ((hi ^ ((l31 >> 1) & 7)) << 4);
    const unsigned aw = L_P1 + 16384 + (8 * hi + q4) * 256 + (((8 * wn + 2 * (G & 1) + (p4 & 1)) ^ (q4 << 2)) << 4) + 8 * (p4 >> 1);
    bf16x8 xf[4][2], wf[4][2];
    auto rd = [&](int ks, unsigned so) {
#pragma unroll
      for (int t = 0; t < 2; ++t) {
        wf[ks][t] = ld_tr2(sm + (((aw ^ (t * 64)) + so) + ks * 4096), sm + (((aw ^ (t * 64)) + so) + ks * 4096 + 1024));
        xf[ks][t] = ld_b128(sm + (((ax ^ (ks * 32)) + so) + t * 4096));
      }
    };
    auto wset8 = [&](int w) { FENCE(); *(volatile LAS char*)(sm + L_W + w) = 1; FENCE(); };
    BAR();
    WAITV(8);
    wset8(W_INIT + wid);
    lds_wait_eq(0u, sm0 + L_W + W_INIT, 0x01010101u);
    STAMP(16);
    rd(0, 0); rd(1, 0);
    unsigned lv = 0;
    SCHED();
#pragma unroll 1
    for (int s = 0; s < 10; ++s) {
      const unsigned so = (s & 3) * 32768, sn = ((s + 1) & 3) * 32768;
#pragma unroll
      for (int ks = 0; ks < 4; ++ks) {
        const int wn1 = W_INIT + 4 * (s < 9 ? s + 1 : 9);
        if (ks == 0) lv = wraw(wn1);
        if (ks == 2) {
          if (s == 0) { WAITV(0); wset8(wn1 + wid); }
          lds_wait_eq(lv, sm0 + L_W + wn1, 0x01010101u);
          STAMP2(17 + s);
          SCHED();
        }
        if (ks < 2) rd(ks + 2, so); else rd(ks - 2, sn);
        if (ks == 1) wset(W_CONS1 + 4 * wid, s + 1);
#pragma unroll
        for (int tj = 0; tj < 2; ++tj)
#pragma unroll
          for (int ti = 0; ti < 2; ++ti) acc[tj][ti] = MFMA32(wf[ks][tj], xf[ks][ti], acc[tj][ti]);
        SGB(0x8, 1); SGB(0x100, 2); SGB(0x8, 1); SGB(0x100, 2); SGB(0x8, 1); SGB(0x100, 2); SGB(0x8, 1);
        SCHED();
      }
    }
    STAMP(1);
    lds_wait_ge(sm0 + L_W + W_STATS, 1);
    const float sc = (c1 < 5) ? QSCALE : 1.f;
#pragma unroll
    for (int ti = 0; ti < 2; ++ti) {
      const int tok = 64 * wt + 32 * ti + l31;
      const f32x2 st = *(LAS f32x2*)(sm + L_STAT + tok * 8);
      const float r = st[1] * sc, nm = -st[0];
#pragma unroll
      for (int tj = 0; tj < 2; ++tj)
#pragma unroll
        for (int hf = 0; hf < 2; ++hf) {
          const int col = 64 * wn + 32 * tj + 16 * hf + 8 * hi;
          const f32x4 ca = *(LAS f32x4*)(sm + L_CS + col * 4), cb = *(LAS f32x4*)(sm + L_CS + col * 4 + 16);
          float v[8];
#pragma unroll
          for (int j = 0; j < 4; ++j) {
            v[j] = (acc[tj][ti][8 * hf + j] + nm * ca[j]) * r;
            v[4 + j] = (acc[tj][ti][8 * hf + 4 + j] + nm * cb[j]) * r;
          }
          u32x4 o = {pk2(v[0], v[1]), pk2(v[2], v[3]), pk2(v[4], v[5]), pk2(v[6], v[7])};
          ST128(o, hi * 2048 + tok * 16, WS_QKV + (unsigned)(g * 240 + 16 * c1 + 8 * wn + 4 * tj + 2 * hf) * 2048);
        }
    }
    {
      const u32x2 e = *(volatile LAS u32x2*)(sm + L_W + W_EPO);
      epoch = ((u64)(unsigned)rfl(e[1]) << 32) | (unsigned)rfl(e[0]);
    }
    WAITV(0);
    arrive_n(1, WS_F1, 4);
  } else {
    const int sw = wid & 1;
    const bool srv = wid >= 6;
    load_epoch();
    u32x4 v[10];
    if (srv) {
      const char* src = sw ? wqkv + (size_t)(40 * g) * 3840 + (c1 < 15 ? c1 : 0) * 256 : x + (size_t)(128 * g + 8 * c1) * 1280;
      const unsigned vs = sw ? G * 3840 + l15 * 16 : (lane >> 3) * 1280 + (lane & 7) * 16, st = sw ? 4 * 3840 : 128;
#pragma unroll
      for (int i = 0; i < 10; ++i) v[i] = *(const u32x4*)(src + i * st + vs);
    }
    SCHED();
    BAR();
    if (srv) {
      STAMP(15);
      if (sw == 0) {
        float s = 0.f, q = 0.f;
#pragma unroll
        for (int i = 0; i < 10; ++i)
#pragma unroll
          for (int j = 0; j < 4; ++j) { s = dot2(v[i][j], 0x3f803f80u, s); q = dot2(v[i][j], v[i][j], q); }
        s = sum8(s); q = sum8(q);
        const float mu = s * (1.f / 640.f);
        const float rstd = __builtin_amdgcn_rsqf(fmaxf(q * (1.f / 640.f) - mu * mu, 0.f) + 1e-5f);
        if ((lane & 3) == 0) ST64((u32x2{__float_as_uint(mu), __float_as_uint(rstd)}), (lane & 56) + (lane & 4) * 16, WS_ST1 + (unsigned)(16 * g + c1) * 128);
      } else {
        float a[8] = {};
#pragma unroll
        for (int i = 0; i < 10; ++i)
#pragma unroll
          for (int j = 0; j < 4; ++j) { a[2 * j] += bflo(v[i][j]); a[2 * j + 1] += bfhi(v[i][j]); }
#pragma unroll
        for (int j = 0; j < 8; ++j) a[j] = sum32(sum16(a[j]));
        if (lane < 32) {
          const f32x4 o = (lane & 16) ? f32x4{a[4], a[5], a[6], a[7]} : f32x4{a[0], a[1], a[2], a[3]};
          __builtin_amdgcn_raw_buffer_store_b128(__builtin_bit_cast(u32x4, o), rs, l15 * 32 + (lane & 16) + WS_CS1 + (unsigned)(c1 * 16 + g) * 512, 0, 16);
        }
      }
      get_epoch();
      WAITV(0);
      arrive_n(0, WS_F0, 2);
      STAMP(16);
    } else {
      get_epoch();
      if (wid < 5) {
        const int o = 5 * g + wid;
        const unsigned stg = L_HSTG + wid * 10240;
        glds5(lane * 16, wo + (size_t)o * 10240, sm0 + stg);
        glds5(lane * 16, wo + (size_t)o * 10240 + 5120, sm0 + stg + 5120);
        WAITV(0);
        const int pos = wid == 2 ? 64 + g : 4 * g + wid - (wid > 2);
        wide_read(stg, WS_W3 + (pos >> 2) * 5120 + (pos & 3) * 256, 100);
#pragma unroll
        for (int j = 0; j < 10; ++j)
          __builtin_amdgcn_raw_buffer_store_b128(__builtin_bit_cast(u32x4, rf[j]), rs, rvo + (j % 5) * 1024 + (j / 5) * rstep5, 0, 16);
        rn = 0;
        WAITV(0);
        arrive_n(1, WS_F1, 5);
      }
    }
    if (wid == 7 && c1 < 15) {
      late_load(7);
      late_load(4);
      wait_flags(ws + WS_F0 + (lane < 16 ? g * 16 + lane : (l15 * 16 + c1)) * 128, epoch);
      const u32x4 gs = LD128((lane >> 2) * 128 + p4 * 16, WS_ST1 + (unsigned)g * 2048);
      f32x4 t = {};
#pragma unroll
      for (int i = 0; i < 8; ++i) t += __builtin_bit_cast(f32x4, LD128(hi * 4096 + l31 * 16, WS_CS1 + (unsigned)(c1 * 16 + i) * 512));
#pragma unroll
      for (int j = 0; j < 4; ++j) t[j] = sum32(t[j]);
      *(LAS u32x4*)(sm + L_STAT + lane * 16) = gs;
      if (lane < 32) *(LAS f32x4*)(sm + L_CS + lane * 16) = t;
      *(volatile LAS u32x2*)(sm + L_W + W_EPO) = u32x2{(unsigned)epoch, (unsigned)(epoch >> 32)};
      wset(W_STATS, 1);
      STAMP(19);
    }
    if (wid >= 4) {
      if (wid != 7 || c1 == 15) late_load(wid);
      if (wid == 6 && c1 < 15) { late_load(5); WAITV(0); wset(W_LL, 1); }
      late_read();
    }
    STAMP(17);
    if (wid == 6) {
      if (c1 < 15) { for (int sp = 0; wget(W_CNT + 4) < 4 && sp < SPIN_CAP; ++sp) __builtin_amdgcn_s_sleep(1); __builtin_amdgcn_s_sleep(GDLY); }
      else wait_flags(ws + WS_HINT + g * 128, epoch);
      wwait(W_S1DONE, 4);
      const int i = lane < 18 ? lane : 0;
      const int which = i / 6, po = which ? (i % 6) >> 1 : 0, e = i & 1;
      const int tile = (640 * which + 40 * c2 + 39 * e) >> 7;
      const int pn = g2 - po < 0 ? 0 : g2 - po;
      wait_flags(ws + WS_F1 + (pn * 16 + tile) * 128, epoch);
      wset(W_GO, 1);
    }
  }
  const char* wsrc3 = ws + WS_W3 + (c & 7) * 102400 + (wid & 1) * 10240;
  const char* wsrc4 = ws + WS_W4 + c * 245760 + (wid & 1) * 10240 - 5 * 20480;
  const char* wsrc5 = ws + WS_W5 + (c & 7) * 245760 + (wid & 1) * 10240 - 17 * 20480;
  auto wissue_half = [&](int st, int h) {
    const char* sb = (st < 5 ? wsrc3 : st < 17 ? wsrc4 : wsrc5) + st * 20480 + h * 5120;
    glds5_sc1(lane * 16, sb, sm0 + L_RING + ((st + 3) % 7) * RSLOT + (wid & 1) * 10240 + h * 5120);
  };
  STAMP(2);

  bool wready;
  {
    const int hs = wid >> 2, qb = wid & 3;
    if (!isC) {
      const int bk = wid & 3, pn = (bk == 3 || g2 - bk < 0) ? g2 : g2 - bk;
      const char* sA = ws + WS_QKV + (size_t)(pn * 240 + (bk == 3 ? 0 : 80) + 5 * c2) * 2048;
      const unsigned dA = sm0 + (bk == 3 ? L_OST : L_K + (2 - bk) * 2048), dstr = bk == 3 ? 2048 : 6144, dB = sm0 + L_V + (2 - bk) * 2048;
      if (wid == 7) {
        if (c1 == 15) wait_flags(ws + WS_F1 + (l15 * 16 + 15) * 128, epoch);
        if (__all(ld_flag(ws + WS_F1 + (l15 * 16 + 15) * 128) == epoch)) wset(W_WREADY, 1);
      }
      lds_wait_ge(sm0 + L_W + W_GO, 1);
      STAMP(3);
#pragma unroll
      for (int j = 0; j < 5; ++j) glds2_sc1(lane * 16, sA + j * 2048, dA + j * dstr);
      if (bk < 3) {
#pragma unroll
        for (int j = 0; j < 5; ++j) glds2_sc1(lane * 16, sA + (80 + j) * 2048, dB + j * VSTR);
      }
    }
    const unsigned ak0 = L_K + (3 * hs + hi) * 6144 + l31 * 16, ak1 = L_K + 2 * 6144 + l31 * 16;
    const int kb_lo = (g2 >= 2) ? qb : (g2 == 1 ? 4 : 8), kb_hi = qb + 8;
    const float NINF = -__builtin_inff();
    f32x16 cD, cF;
#pragma unroll
    for (int r = 0; r < 16; ++r) {
      const bool low = (r & 3) + 8 * (r >> 2) + 4 * hi <= l31;
      cD[r] = low ? 0.f : NINF;
      cF[r] = low ? NINF : 0.f;
    }
    const bool isOne = (G & 1) && p4;
    const unsigned av0 = isOne ? L_ONES
                               : L_V + (4 * hi + q4) * 16 + ((G & 1) ? 2 * VSTR + 8 * hs : (3 * hs + (p4 & 1)) * VSTR + 8 * (p4 >> 1));
    const unsigned avs = isOne ? 0 : 512;
    unsigned a0 = ak0 + qb * 512, a1 = hi ? L_KT + qb * 16 : ak1 + qb * 512, av = av0 + qb * avs;
    const unsigned a1s = hi ? 16 : 512;
    asm volatile("" : "+v"(cD), "+v"(cF), "+v"(a0), "+v"(a1), "+v"(av));
    if (wid >= 4 && wid < 7) WAITV(10); else WAITV(0);
    BAR();
    STAMP(4);
    wready = wget(W_WREADY);
    const bool wiss = wready && (wid >> 1) == 1;
    const int row = 32 * qb + l31;
    u32x4 q0 = *(LAS u32x4*)(sm + L_OST + (3 * hs + hi) * 2048 + row * 16), q1 = *(LAS u32x4*)(sm + L_OST + 2 * 2048 + row * 16);
    if (hi || hs) { q1[0] = 0; q1[1] = 0; }
    if (hi || !hs) { q1[2] = 0; q1[3] = 0; }
    const bf16x8 qf0 = __builtin_bit_cast(bf16x8, q0), qf1 = __builtin_bit_cast(bf16x8, q1);
    auto vmax = [&](const f32x16& s, float m) {
#pragma unroll
      for (int r = 0; r < 16; r += 2) m = fmaxf(fmaxf(m, s[r]), s[r + 1]);
      return m;
    };
    f32x16 o;
    float rl, m;
    auto finish = [&]() {
      auto sw = __builtin_amdgcn_permlane32_swap(__float_as_uint(o[8]), __float_as_uint(o[8]), false, false);
      const float l = __uint_as_float(sw[1]);
      rl = __builtin_amdgcn_rcpf(l);
      float am = 0.f;
#pragma unroll
      for (int r = 0; r < 12; r += 2) am = fmaxf(fmaxf(am, fabsf(o[r])), fabsf(o[r + 1]));
      return (bool)__all(l <= 0x1p30f && l >= 0x1p-30f && am < __builtin_inff());
    };
    {
      f32x16 s[2];
      bf16x8 k0 = ld_b128(sm + a0 + 8 * 512), k1 = ld_b128(sm + a1 + 8 * a1s);
      s[1] = mfma32_c(k0, qf0, cD);
      s[0] = mfma32_c(k1, qf1, s[1]);
      asm volatile("s_nop 11" : "+v"(s[0]));
      m = max32(vmax(s[0], NINF));
      u32x4 q2 = q1;
      if (hi) q2[0] = pk2(-m, -3.0e38f);
      const bf16x8 qf2 = __builtin_bit_cast(bf16x8, q2);
      s[0] = MFMA32(k1, qf2, s[1]);
      k0 = ld_b128(sm + a0); k1 = ld_b128(sm + a1);
      o = f32x16{};
      SCHED();
      WAITV(0);
      BAR();
      SCHED();
#pragma unroll
      for (int j = 0; j < 9; ++j) {
        if (j == 0 && hs) __builtin_amdgcn_s_setprio(1);
        if (j == 6 && hs) __builtin_amdgcn_s_setprio(0);
        f32x16& sc = s[j & 1];
        f32x16& sn = s[(j + 1) & 1];
        unsigned avj = av + (j == 0 ? 8 : j - 1) * avs;
        bf16x8 vf0 = ld_tr2(sm + avj, sm + avj + 128), vf1 = ld_tr2(sm + avj + 256, sm + avj + 384);
        unsigned pw[8];
        if (j < 8) sn = MFMA32(k0, qf0, j == 0 ? cF : f32x16{});
        SCHED();
#pragma unroll
        for (int r = 0; r < 2; ++r) pw[r] = pk2(__builtin_amdgcn_exp2f(sc[2 * r]), __builtin_amdgcn_exp2f(sc[2 * r + 1]));
        SCHED();
        if (j < 8) sn = MFMA32(k1, qf2, sn);
        if (j < 7) { a1 += a1s; k0 = ld_b128(sm + a0 + (j + 1) * 512); k1 = ld_b128(sm + a1); }
        SCHED();
#pragma unroll
        for (int r = 2; r < 5; ++r) pw[r] = pk2(__builtin_amdgcn_exp2f(sc[2 * r]), __builtin_amdgcn_exp2f(sc[2 * r + 1]));
        SCHED();
        o = MFMA32(vf0, __builtin_bit_cast(bf16x8, (u32x4{pw[0], pw[1], pw[2], pw[3]})), o);
        SCHED();
#pragma unroll
        for (int r = 5; r < 8; ++r) pw[r] = pk2(__builtin_amdgcn_exp2f(sc[2 * r]), __builtin_amdgcn_exp2f(sc[2 * r + 1]));
        SCHED();
        o = MFMA32(vf1, __builtin_bit_cast(bf16x8, (u32x4{pw[4], pw[5], pw[6], pw[7]})), o);
        SCHED();
        if (j < 5 && 2 * j < rn) { retile_store(2 * j); retile_store(2 * j + 1); }
        if (j < 8 && wiss) wissue_half(j >> 1, j & 1);
      }
    }
    if (!finish()) {
      int lq = l31 - 4 * hi;
      OPAQUE(lq);
#pragma unroll 1
      for (int k2 = kb_lo; k2 < kb_hi; ++k2) {
        f32x16 cz;
#pragma unroll
        for (int r = 0; r < 16; ++r) cz[r] = (k2 == qb && (r & 3) + 8 * (r >> 2) <= lq) ? NINF : 0.f;
        f32x16 s = MFMA32(ld_b128(sm + ak0 + k2 * 512), qf0, cz);
        s = MFMA32(ld_b128(sm + ak1 + k2 * 512), qf1, s);
        m = vmax(s, m);
      }
      m = max32(m);
      o = f32x16{};
#pragma unroll 1
      for (int kb = kb_lo; kb <= kb_hi; ++kb) {
        f32x16 cin;
#pragma unroll
        for (int r = 0; r < 16; ++r) cin[r] = ((r & 3) + 8 * (r >> 2) <= lq ? kb == qb : kb == kb_hi) ? NINF : -m;
        f32x16 s = MFMA32(ld_b128(sm + ak0 + kb * 512), qf0, cin);
        s = MFMA32(ld_b128(sm + ak1 + kb * 512), qf1, s);
        const unsigned av = av0 + kb * avs;
        bf16x8 vf0 = ld_tr2(sm + av, sm + av + 128), vf1 = ld_tr2(sm + av + 256, sm + av + 384);
        unsigned pw[8];
#pragma unroll
        for (int r = 0; r < 8; ++r) pw[r] = pk2(__builtin_amdgcn_exp2f(s[2 * r]), __builtin_amdgcn_exp2f(s[2 * r + 1]));
        o = MFMA32(vf0, __builtin_bit_cast(bf16x8, (u32x4{pw[0], pw[1], pw[2], pw[3]})), o);
        o = MFMA32(vf1, __builtin_bit_cast(bf16x8, (u32x4{pw[4], pw[5], pw[6], pw[7]})), o);
      }
      (void)finish();
    }
    const unsigned ob = WS_O + (unsigned)(g2 * 80) * 2048;
    ST128((u32x4{pk2(o[0] * rl, o[1] * rl), pk2(o[2] * rl, o[3] * rl), pk2(o[4] * rl, o[5] * rl), pk2(o[6] * rl, o[7] * rl)}),
          hi * 2048 + row * 16, ob + (4 * c2 + 2 * hs) * 2048);
    if (!hi) ST64((u32x2{pk2(o[8] * rl, o[9] * rl), pk2(o[10] * rl, o[11] * rl)}), row * 8, ob + (64 + c2) * 2048 + hs * 1024);
    STAMP(5);
    WAITV(0);
    if (count(3) == 7) {
      raise(WS_F2 + (g2 * 16 + c2) * 128);
      if (lane == 0) st_flag(ws + WS_F1P + wgid * 8, epoch);
    }
    STAMP(6);
    if (isC) wwait(W_CNT + 12, 8);
  }

  if (isL) {
    int issued = 4;
    if (!wready) {
#pragma unroll 1
      for (int i = 0; i < 1; ++i) wait_flags(ws + WS_F1 + (l15 * 16 + 15) * 128, epoch);
      issued = 0;
    }
    const int wl = W_LANDW + 2 * (wid & 1);
    wwait(W_CNT + 12, 8);
    if (wready) wset16(wl, 4);
    for (; issued < 5; ++issued) { const int st = rfl(issued); wissue_half(st, 0); wissue_half(st, 1); }
    WAITV(0);
    wset16(wl, 5);
    __builtin_amdgcn_s_sleep(LDLY);
    {
      u64 f[4];
#pragma unroll
      for (int i = 0; i < 4; ++i) f[i] = ld_flag(ws + WS_F1P + i * 512 + lane * 8);
#pragma unroll
      for (int i = 0; i < 4; ++i) wait_flags_from(ws + WS_F1P + i * 512 + lane * 8, f[i], epoch);
    }
    STAMP(30);
    if (wid == 4) wset(W_GO, 2);
    const char* p = wsrc4 + 5 * 20480;
    unsigned dst = sm0 + L_RING + RSLOT + (wid & 1) * 10240;
#pragma unroll 1
    for (int st = 5; st < 29; ++st) {
      if (st == 7 + P3REL) { WAITV(0); wset16(wl, st); }
      for (int sp = 0; wget4(W_CONSW) < (st < 7 ? 1 : st - 5) && sp < SPIN_CAP; ++sp) __builtin_amdgcn_s_sleep(1);
      FENCE();
      glds10_sc1(lane * 16, p, dst);
      p = st == 16 ? wsrc5 + 17 * 20480 : p + 20480;
      dst = dst + RSLOT >= sm0 + L_RING + 7 * RSLOT ? dst - 6 * RSLOT : dst + RSLOT;
      WAITV(20);
      if (st >= 7 && (st < 7 + P3REL || st > 8 + P3REL)) wset16(wl, st - 1);
      STAMP(31 + st - 1);
    }
    WAITV(10);
    wset16(wl, 28);
    WAITV(0);
    wset16(wl, 29);
    STAMP(60);
    STAMP_DUMP();
    return;
  }
  if (!isC) {
    if (wid == 6) {
#pragma unroll 1
      for (int k = 0; k < 2; ++k) {
        for (int sp = 0; (k < 1 ? wget4(W_DONE) <= k : wget(W_CNT + 20) < 4) && sp < SPIN_CAP; ++sp) __builtin_amdgcn_s_sleep(2);
        __builtin_amdgcn_s_sleep(PDLY);
        wait_flags(ws + WS_F4 + k * FL + (g * 16 + l15) * 128, epoch);
        wset(W_RDY, k + 3);
        STAMP(22 + k);
      }
    } else {
      for (int sp = 0; wget(W_GO) < 2 && sp < SPIN_CAP; ++sp) __builtin_amdgcn_s_sleep(4);
      FENCE();
      if (lane < 48) {
        f32x4 t = {};
#pragma unroll
        for (int i = 0; i < 27; ++i) t += __builtin_bit_cast(f32x4, LD128(lane * 16, WS_CSUM + (unsigned)(i * 12288 + c * 768)));
#pragma unroll
        for (int e = 0; e < 4; ++e) {
          const unsigned ch = pk2(t[e], t[e]);
          *(LAS u32x4*)(sm + L_WX + lane * 64 + e * 16) = u32x4{ch, pk2(t[e] - bflo(ch), 0.f), 0, 0};
        }
      }
      wset(W_WREADY, 2);
#pragma unroll 1
      for (int k = 0; k < 1; ++k) {
        for (int sp = 0; wget4(W_DONE) <= k && sp < SPIN_CAP; ++sp) __builtin_amdgcn_s_sleep(1);
        FENCE();
        raise(WS_F4 + k * FL + wgid * 128);
        STAMP(22 + k);
      }
      if (b == 0) {
#pragma unroll 1
        for (int i = 0; i < 4; ++i) wait_flags(ws + WS_F2 + (i * 64 + lane) * 128, epoch);
        if (lane == 0) st_flag(ws + WS_EPOCH, epoch);
      }
    }
    STAMP_DUMP();
    return;
  }

  const int wdn = W_DONE + 4 * wid;
  const int wc = W_CONSW + 4 * wid;
  const unsigned lane16 = lane * 16;

  const int r3 = 64 * (c >> 3) + 16 * wid + l15;
  {
    unsigned hpk[10];
    const char* xp = x + (size_t)(128 * g + r3) * 1280 + (c & 7) * 160;
    const u32x4 xr0 = *(const u32x4*)(xp + G * 16), xr1 = *(const u32x4*)(xp + 64 + G * 16);
    const u32x2 xr2 = *(const u32x2*)(xp + 128 + G * 8);
    u32x4 xf[20];
    auto ldo = [&](int s) {
      const unsigned so = WS_O + (unsigned)(g * 80 + 4 * s) * 2048;
      if (s < 16) xf[s] = LD128(G * 2048 + r3 * 16, so);
      else { const u32x2 a = LD64(G * 2048 + r3 * 8, so), d = LD64(G * 2048 + r3 * 8 + 1024, so); xf[s] = u32x4{a[0], a[1], d[0], d[1]}; }
    };
    f32x4 acc[5] = {};
    {
      constexpr int NB = 15;
      bf16x8 wf[NB];
      unsigned so = 0, lv = 0;
      auto rd = [&](int f) {
        if (f == 80) wwait2(W_LANDW, lv, 5);
        if (f % 20 == 0) { so = lane16; OPAQUE(so); so += slot_of(f / 20); OPAQUE(so); }
        wf[f % NB] = ld_b128(sm + so + (f % 20) * 1024);
        if (f == 64) lv = wraw(W_LANDW);
      };
      __builtin_amdgcn_s_sleep(PDLY - 4);
      wwait2(W_LANDW, 0u, 4);
#pragma unroll
      for (int f = 0; f < NB; ++f) rd(f);
      SCHED();
      wait_flags(ws + WS_F2 + (g * 16 + l15) * 128, epoch);
      STAMP(7);
#pragma unroll
      for (int s = 0; s < XW3; ++s) ldo(s);
      SCHED();
#pragma unroll
      for (int f = 0; f < 100; ++f) {
        acc[f % 5] = MFMA16(wf[f % NB], __builtin_bit_cast(bf16x8, xf[f / 5]), acc[f % 5]);
        SCHED();
        if (f + NB < 100) rd(f + NB);
        if (f % 3 == 0 && XW3 + f / 3 < 20) ldo(XW3 + f / 3);
        SCHED();
      }
    }
    STAMP(8);
    float s1 = 0.f, s2 = 0.f;
#pragma unroll
    for (int d = 0; d < 10; ++d) {
      const unsigned xv = d < 4 ? xr0[d & 3] : d < 8 ? xr1[d & 3] : xr2[d & 1];
      const unsigned a = pk2(acc[d >> 1][2 * (d & 1)], acc[d >> 1][2 * (d & 1) + 1]);
      const unsigned h = pk2(bflo(a) + bflo(xv), bfhi(a) + bfhi(xv));
      hpk[d] = h;
      s1 += bflo(h) + bfhi(h);
      s2 += bflo(h) * bflo(h) + bfhi(h) * bfhi(h);
    }
    const unsigned hb = WS_H + (unsigned)(g * 80 + 10 * (c & 7)) * 2048;
    ST128((u32x4{hpk[0], hpk[1], hpk[2], hpk[3]}), G * 2048 + r3 * 16, hb);
    ST128((u32x4{hpk[4], hpk[5], hpk[6], hpk[7]}), G * 2048 + r3 * 16, hb + 4 * 2048);
    {
      auto a0 = __builtin_amdgcn_permlane16_swap(hpk[8], hpk[8], false, false);
      auto a1 = __builtin_amdgcn_permlane16_swap(hpk[9], hpk[9], false, false);
      if (!(G & 1)) ST128((u32x4{a0[0], a1[0], a0[1], a1[1]}), (G >> 1) * 2048 + r3 * 16, hb + 8 * 2048);
    }
    s1 = sum32(sum16(s1)); s2 = sum32(sum16(s2));
    if (G == 0) ST64((u32x2{__float_as_uint(s1), __float_as_uint(s2)}), r3 * 8, WS_STAT + (unsigned)(g * 8 + (c & 7)) * 1024);
    wset(wc, 1 + P3REL);
    *(LAS u32x4*)(sm + L_OST + wid * 2560 + lane * 16) = u32x4{hpk[0], hpk[1], hpk[2], hpk[3]};
    *(LAS u32x4*)(sm + L_OST + wid * 2560 + 1024 + lane * 16) = u32x4{hpk[4], hpk[5], hpk[6], hpk[7]};
    *(LAS u32x2*)(sm + L_OST + wid * 2560 + 2048 + lane * 8) = u32x2{hpk[8], hpk[9]};
    WAITV(0);
    arrive_w(4, WS_F3);
    STAMP(9);
  }

  {
    constexpr int NB = 6;
    bf16x8 wf[NB];
    unsigned so = 0;
    unsigned lv = 0;
    auto rd = [&](int n) __attribute__((always_inline)) {
      const int part = n / 123, i = n % 123, f = 120 * part + i;
      if (i >= 120) {
        if (n == 120) lds_wait_ge(sm0 + L_W + W_WREADY, 2);
        wf[n % NB] = ld_b128(sm + L_WX + (3 * part + i - 120) * 512 + (lane16 & 511));
      } else {
      if (f % 20 == 0) { wwait2(W_LANDW, lv, 5 + f / 20 + 1); so = lane16; OPAQUE(so); so += slot_of(5 + f / 20); OPAQUE(so); }
      wf[n % NB] = ld_b128(sm + so + (f % 20) * 1024);
      if (f % 20 == 12) lv = wraw(W_LANDW);
      if (f % 20 == 19 && f < 239) wset(wc, 5 + f / 20 + 2);
      }
    };
    __builtin_amdgcn_s_sleep(PDLY - 2);
#pragma unroll
    for (int n = 0; n < NB; ++n) rd(n);
    SCHED();
    wait_flags(ws + WS_F3 + (g * 16 + l15) * 128, epoch);
    STAMP(10);
    const int r4 = 32 * wid + l31;
    const unsigned vx = hi * 2048 + r4 * 16;
    const unsigned hb = WS_H + (unsigned)(g * 80) * 2048;
    u32x4 xf[40];
    u32x2 stp[4];
    constexpr int XW = XW4;
#pragma unroll
    for (int i = 0; i < XW; ++i) xf[i] = LD128(vx, hb + i * 4096);
    SCHED();
    float r2 = 0.f;
    unsigned xe0 = 0, xe1 = 0;
    static_for<0, 2>([&](auto pc) __attribute__((always_inline)) {
      constexpr int part = pc.value;
      f32x16 acc[3] = {};
      static_for<0, 123>([&](auto ic) __attribute__((always_inline)) {
        constexpr int i = ic.value;
        const int n = 123 * part + i;
        if (i % 20 == 0 && n > 0 && i < 120) { STAMP2(42 + (120 * part + i) / 20); SCHED(); }
        acc[i % 3] = MFMA32(wf[n % NB], __builtin_bit_cast(bf16x8, i < 120 ? xf[i < 120 ? i / 3 : 0] : u32x4{xe0, xe1, 0, 0}), acc[i % 3]);
        SCHED();
        if (n + NB < 246) rd(n + NB);
        if (part == 0 && (i & 1) && (i >> 1) + XW < 40) xf[(i >> 1) + XW] = LD128(vx, hb + ((i >> 1) + XW) * 4096);
        if (n >= 4 && n < 12 && !(n & 1)) stp[(n - 4) >> 1] = LD64(hi * 4096 + r4 * 8, WS_STAT + (unsigned)(g * 8 + ((n - 4) >> 1)) * 1024);
        if (n == 44) {
          float s1 = 0.f, s2 = 0.f;
#pragma unroll
          for (int j = 0; j < 4; ++j) { s1 += __uint_as_float(stp[j][0]); s2 += __uint_as_float(stp[j][1]); }
          s1 = sum32(s1) * (1.f / 640.f); s2 = sum32(s2) * (1.f / 640.f);
          r2 = __builtin_amdgcn_rcpf(fmaxf(s2 - s1 * s1, 0.f) + 1e-5f);
          const unsigned mh = pk2(-s1, 0.f);
          xe0 = hi ? 0u : pk2(-s1, -s1 - bflo(mh));
          xe1 = hi ? 0u : mh;
          OPAQUE(r2); OPAQUE(xe0); OPAQUE(xe1);
        }
        SCHED();
        if (part == 1 && i == 36) { WAITV(0); wset(wdn, 1); SCHED(); }
      });
      STAMP2(54 + part);
      if (part == 1) STAMP(11);
#pragma unroll
      for (int t = 0; t < 3; ++t) {
        unsigned o[4];
#pragma unroll
        for (int d = 0; d < 4; ++d) {
          const f32x2 hv = (f32x2{acc[t][2 * d], acc[t][2 * d + 1]} * r2) * f32x2{acc[t][8 + 2 * d], acc[t][8 + 2 * d + 1]};
          o[d] = pk2(hv[0], hv[1]);
        }
        ST128((u32x4{o[0], o[1], o[2], o[3]}), vx, WS_HH + (unsigned)(g * 192 + 96 * part + 6 * c + 2 * t) * 2048);
      }
      SCHED();
    });
    wset(wc, 18);
    STAMP(12);
  }

  {
    unsigned ln5 = lane16;
    OPAQUE(ln5);
    const int wk = wid & 1;
    const unsigned hb5 = WS_HH + (unsigned)(g * 192) * 2048 + wk * 16384;
    const unsigned v50 = (ln5 >> 8) * 2048 + (64 * (c >> 3) + 16 * wid) * 16 + (ln5 & 255), v51 = v50 ^ 256;
    f32x4 acc[5] = {}, acp[5] = {};
    constexpr int NB = 8;
    bf16x8 wf[NB];
    u32x4 x5[24], h0, h1;
    u32x2 h2;
    unsigned so = 0;
    unsigned lv = 0;
    auto rd = [&](int e) {
      if (e % 10 == 0) { wwait2(W_LANDW, lv, 17 + e / 10 + 1); so = ln5 + wk * 10240; OPAQUE(so); so += slot_of(17 + e / 10); OPAQUE(so); }
      wf[e % NB] = ld_b128(sm + so + (e % 10) * 1024);
      if (e % 10 == 3) lv = wraw(W_LANDW);
      if (e % 10 == 9) wset(wc, 17 + e / 10 + 2);
    };
    auto ldx = [&](int part, int j) {
      const int kk = 12 * part + (j >> 1);
      x5[j] = LD128((j & 1) ? v51 : v50, hb5 + (4 * (kk >> 1) + (kk & 1)) * 8192);
    };
    lds_wait_ge(sm0 + L_W + W_RDY, 3);
    STAMP(40);
#pragma unroll
    for (int j = 0; j < XW5; ++j) ldx(0, j);
#pragma unroll
    for (int e = 0; e < NB; ++e) rd(e);
    SCHED();
    WAITV(XW5);
    arrive_w(5, WS_F4 + FL);
    STAMP(13);
    SCHED();
#pragma unroll
    for (int part = 0; part < 2; ++part) {
#pragma unroll
      for (int i = 0; i < 60; ++i) {
        const int e = 60 * part + i;
        acc[i % 5] = MFMA16(wf[e % NB], __builtin_bit_cast(bf16x8, x5[2 * (i / 5)]), acc[i % 5]);
        SCHED();
        acp[i % 5] = MFMA16(wf[e % NB], __builtin_bit_cast(bf16x8, x5[2 * (i / 5) + 1]), acp[i % 5]);
        SCHED();
        if (e + NB < 120) rd(e + NB);
        if (i % 2 == 0 && XW5 + i / 2 < 24) ldx(part, XW5 + i / 2);
        SCHED();
      }
      if (part == 0) {
        lds_wait_ge(sm0 + L_W + W_RDY, 4);
        STAMP(41);
#pragma unroll
        for (int j = 0; j < XW5; ++j) ldx(1, j);
        h0 = *(LAS u32x4*)(sm + L_OST + wid * 2560 + ln5); h1 = *(LAS u32x4*)(sm + L_OST + wid * 2560 + 1024 + ln5);
        h2 = *(LAS u32x2*)(sm + L_OST + wid * 2560 + 2048 + (ln5 >> 1));
        for (int sp = 0; wget4(W_CONSW) < 24 && sp < SPIN_CAP; ++sp) {}
        FENCE();
        SCHED();
      }
    }
#pragma unroll
    for (int tt = 0; tt < 5; ++tt) *(LAS f32x4*)(sm + slot_of(22) + (wid ^ 1) * 5120 + tt * 1024 + ln5) = acp[tt];
    wset(wdn, 9);
    lds_wait_ge(sm0 + L_W + W_DONE + 4 * (wid ^ 1), 9);
#pragma unroll
    for (int tt = 0; tt < 5; ++tt) acc[tt] += *(LAS f32x4*)(sm + slot_of(22) + wid * 5120 + tt * 1024 + ln5);
    STAMP(14);
    unsigned opk[10];
#pragma unroll
    for (int d = 0; d < 10; ++d) {
      const unsigned hv = d < 4 ? h0[d & 3] : d < 8 ? h1[d & 3] : h2[d & 1];
      const unsigned a = pk2(acc[d >> 1][2 * (d & 1)], acc[d >> 1][2 * (d & 1) + 1]);
      opk[d] = pk2(bflo(a) + bflo(hv), bfhi(a) + bfhi(hv));
    }
    const unsigned G5 = ln5 >> 8;
    char* op = out + (size_t)(128 * g + 64 * (c >> 3) + 16 * wid) * 1280 + (c & 7) * 160 + (ln5 & 255) * 80;
    *(u32x4*)(op + G5 * 16) = u32x4{opk[0], opk[1], opk[2], opk[3]};
    *(u32x4*)(op + 64 + G5 * 16) = u32x4{opk[4], opk[5], opk[6], opk[7]};
    *(u32x2*)(op + 128 + G5 * 8) = u32x2{opk[8], opk[9]};
    STAMP(15);
    STAMP_DUMP();
  }
}

extern "C" void kernel_launch(void* const* d_in, const int* in_sizes, int n_in, void* d_out, int out_size, void* d_ws, size_t ws_size,
                              hipStream_t stream) {
  fused_block<<<256, 512, 0, stream>>>((const char*)d_in[0], (const char*)d_in[1], (const char*)d_in[2], (const char*)d_in[4],
                                       (const char*)d_in[5], (const char*)d_in[6], (char*)d_out, (char*)d_ws);
}
```
